```python
import torch

def _softmax(x, axis=-1):
    return torch.softmax(x, dim=axis)
def _gelu(x):
    return torch.nn.functional.gelu(x, approximate="tanh")
def _silu(x):
    return torch.nn.functional.silu(x)
def _sigmoid(x):
    return torch.sigmoid(x)
def _layernorm(x, axis=-1, eps=1e-5):
    m = x.mean(dim=axis, keepdim=True)
    v = x.var(dim=axis, keepdim=True, unbiased=False)
    return (x - m) / torch.sqrt(v + eps)
def _scatter_add(tgt, idx, src, axis=0):
    return tgt.clone().index_add_(axis, idx, src)
def _segment_sum(src, seg, n):
    out = torch.zeros((n,) + src.shape[1:], dtype=src.dtype)
    return out.index_add_(0, seg, src)
def _pad(x, pw):
    flat = tuple(v for pair in reversed(pw) for v in pair)
    return torch.nn.functional.pad(x, flat)

kernel_name = 'gated_mix__grouped_kv_mqa__glu_gated_bilinear__prompt'

def setup_inputs():

    _w = (lambda a: (lambda t: t.to(torch.bfloat16) if t.is_floating_point() else t)(torch.as_tensor(a)))
    def _A_setup():
        B, S, D, H = 1, 2048, 448, 16
        Dh = D // H
        return {
            "x": _w((lambda *s: torch.randn(*s, dtype=torch.bfloat16) * 0.05)(B, S, D)),
            "wq": _w((lambda *s: torch.randn(*s, dtype=torch.bfloat16) * 0.8)(D, H * Dh)),
            "wk": _w((lambda *s: torch.randn(*s, dtype=torch.bfloat16) * 0.8)(D, Dh)),
            "wv": _w((lambda *s: torch.randn(*s, dtype=torch.bfloat16) * 0.05)(D, Dh)),
            "wo": _w((lambda *s: torch.randn(*s, dtype=torch.bfloat16) * 0.05)(H * Dh, D)),
        }
    _A_in = _A_setup()

    def _B_setup():
        B, S, D, F = 1, 2048, 448, 1792
        return {
            "x": _w((lambda *s: torch.randn(*s, dtype=torch.bfloat16) * 0.05)(B, S, D)),
            "w_gate": _w((lambda *s: torch.randn(*s, dtype=torch.bfloat16) * 0.8)(D, F) * (D ** -0.5)),
            "w_up": _w((lambda *s: torch.randn(*s, dtype=torch.bfloat16) * 0.8)(D, F) * (D ** -0.5)),
            "w_down": _w((lambda *s: torch.randn(*s, dtype=torch.bfloat16) * 0.8)(F, D) * (F ** -0.5)),
        }
    _B_in = _B_setup()

    return {
        "A_x": _A_in["x"],
        "A_wq": _A_in["wq"],
        "A_wk": _A_in["wk"],
        "A_wv": _A_in["wv"],
        "A_wo": _A_in["wo"],
        "B_x": _B_in["x"],
        "B_w_gate": _B_in["w_gate"],
        "B_w_up": _B_in["w_up"],
        "B_w_down": _B_in["w_down"],
        "gate_w": _w((lambda *s: torch.randn(*s, dtype=torch.bfloat16) * 0.05)(448) * 16.0),
    }

def reference(A_x, A_wq, A_wk, A_wv, A_wo, B_x, B_w_gate, B_w_up, B_w_down, gate_w):
    def _A_ref(x, wq, wk, wv, wo):
        B, S, D = x.shape
        H = 16
        Dh = D // H
        q = (lambda x, *d: x.permute(*d))((x @ wq).reshape(B, S, H, Dh), 0, 2, 1, 3)
        k = x @ wk
        v = x @ wv
        scores = torch.einsum("bhqd,bkd->bhqk", q, k) * (Dh ** -0.5)
        p = _softmax(scores, axis=-1)
        o = torch.einsum("bhqk,bkd->bhqd", p, v)
        return (lambda x, *d: x.permute(*d))(o, 0, 2, 1, 3).reshape(B, S, H * Dh) @ wo

    def _B_ref(x, w_gate, w_up, w_down):
        g = torch.einsum("bsd,df->bsf", x, w_gate)
        u = torch.einsum("bsd,df->bsf", x, w_up)
        h = g * u
        return torch.einsum("bsf,fd->bsd", h, w_down)

    x = A_x
    g = _sigmoid((x * gate_w).sum(-1, keepdims=True))
    a = _A_ref(x, A_wq, A_wk, A_wv, A_wo)
    b = _B_ref(x, B_w_gate, B_w_up, B_w_down)
    return g * a + (1 - g) * b
```

```cpp
#include <hip/hip_runtime.h>
#include <hip/hip_bf16.h>
#include <stdint.h>

typedef __bf16 bf16x8 __attribute__((ext_vector_type(8)));
typedef __bf16 bf16x2 __attribute__((ext_vector_type(2)));
typedef short s16x4 __attribute__((ext_vector_type(4)));
typedef float f32x16 __attribute__((ext_vector_type(16)));
typedef float f32x4 __attribute__((ext_vector_type(4)));
typedef unsigned u32x4 __attribute__((ext_vector_type(4)));
typedef unsigned u32x2 __attribute__((ext_vector_type(2)));
typedef unsigned long long u64;
typedef __amdgpu_buffer_rsrc_t rsrc_t;
#define LDSP __attribute__((address_space(3)))
#define DEV __device__ __forceinline__
#define SB() __builtin_amdgcn_sched_barrier(0)

constexpr unsigned WS_EPOCH = 0;
#define FSTR 64
#define HPRIO 1
#define PPRIO 0
#define APRIO 0
#define KVFIRST 0
#define P3PD 6
#define SLP 4
constexpr unsigned WS_FLAGS = 11829248;
constexpr unsigned WS_FKV = WS_FLAGS;
constexpr unsigned WS_FH = WS_FKV + 128 * FSTR;
constexpr unsigned WS_FO = WS_FH + 896 * FSTR;
constexpr unsigned WS_FW = WS_FO + 1024 * FSTR;
constexpr unsigned WS_KV = 65536;
constexpr unsigned WS_O = 327680;
constexpr unsigned WS_W3 = 2424832;
constexpr unsigned WS_H = 4489216;
constexpr unsigned WS_END = WS_FLAGS;
constexpr unsigned PROF_OFF = 12582912;
constexpr unsigned L_X = 0;
constexpr unsigned L_WQ = 114688;
constexpr unsigned L_QEX = 143360;
constexpr unsigned L_BS = 151552;
constexpr unsigned L_CNT = 159744;
constexpr unsigned L_FB = 160000 - 160000 % 256 + 256;
constexpr unsigned L_OMG = L_FB + 2048;
constexpr unsigned L_REDA = 0;
constexpr unsigned L_REDB = 65536;
constexpr unsigned LDS_BYTES = 163840;
constexpr int AUX_SC1 = 16;
#define NTB 8
constexpr unsigned OOB = 0x80000000u;

#define STAMP(e)
#define STAMPC(e)
#define STAMPS_OUT()

DEV unsigned opq(unsigned v) { asm volatile("" : "+v"(v)); return v; }
DEV LDSP char* lp(unsigned a) { return (LDSP char*)(size_t)a; }
DEV rsrc_t mkrs(const void* p, unsigned bytes) { return __builtin_amdgcn_make_buffer_rsrc((void*)p, (short)0, (int)bytes, 0x00020000); }
DEV u32x4 ld16(rsrc_t r, unsigned off) { return __builtin_amdgcn_raw_buffer_load_b128(r, off, 0, 0); }
DEV u32x4 ld16c(rsrc_t r, unsigned off) { return __builtin_amdgcn_raw_buffer_load_b128(r, off, 0, AUX_SC1); }
DEV void st16c(rsrc_t r, unsigned off, u32x4 v) { __builtin_amdgcn_raw_buffer_store_b128(v, r, off, 0, AUX_SC1); }
DEV unsigned pk2(float a, float b) { bf16x2 v = {(__bf16)a, (__bf16)b}; return __builtin_bit_cast(unsigned, v); }
DEV bf16x8 asb(u32x4 v) { return __builtin_bit_cast(bf16x8, v); }
DEV f32x16 mfma(bf16x8 a, bf16x8 b, f32x16 c) { return __builtin_amdgcn_mfma_f32_32x32x16_bf16(a, b, c, 0, 0, 0); }
DEV u32x4 packf(const f32x16& a, int s2) {
  u32x4 r;
  r[0] = pk2(a[8 * s2 + 0], a[8 * s2 + 1]); r[1] = pk2(a[8 * s2 + 2], a[8 * s2 + 3]);
  r[2] = pk2(a[8 * s2 + 4], a[8 * s2 + 5]); r[3] = pk2(a[8 * s2 + 6], a[8 * s2 + 7]);
  return r;
}
DEV u32x4 lds16(LDSP char* p) { return *(LDSP u32x4*)p; }
DEV void lds16w(LDSP char* p, u32x4 v) { *(LDSP u32x4*)p = v; }
DEV bf16x8 trfrag(LDSP char* p) {
  s16x4 a = __builtin_amdgcn_ds_read_tr16_b64_v4i16((LDSP s16x4*)p);
  s16x4 b = __builtin_amdgcn_ds_read_tr16_b64_v4i16((LDSP s16x4*)(p + 256));
  u32x2 ua = __builtin_bit_cast(u32x2, a), ub = __builtin_bit_cast(u32x2, b);
  u32x4 r = {ua[0], ua[1], ub[0], ub[1]};
  return asb(r);
}
DEV float xor32(float v) { return __builtin_bit_cast(float, __builtin_amdgcn_ds_bpermute(((threadIdx.x & 63) ^ 32) << 2, __builtin_bit_cast(int, v))); }
DEV float wave_max(float v) {
#pragma unroll
  for (int m = 32; m >= 1; m >>= 1) v = fmaxf(v, __builtin_bit_cast(float, __builtin_amdgcn_ds_bpermute(((threadIdx.x & 63) ^ m) << 2, __builtin_bit_cast(int, v))));
  return v;
}
DEV void poll(const char* ws, unsigned off, bool active, u64 tag) {
  for (;;) {
    u64 v = tag;
    if (active) v = __hip_atomic_load((const u64*)(ws + off), __ATOMIC_RELAXED, __HIP_MEMORY_SCOPE_AGENT);
    if (__all(v == tag)) break;
    __builtin_amdgcn_s_sleep(SLP);
  }
  asm volatile("" ::: "memory");
}
DEV void poll2(const char* ws, unsigned off1, bool act1, unsigned off2, bool act2, u64 tag) {
  for (;;) {
    u64 v1 = tag, v2 = tag;
    if (act1) v1 = __hip_atomic_load((const u64*)(ws + off1), __ATOMIC_RELAXED, __HIP_MEMORY_SCOPE_AGENT);
    if (act2) v2 = __hip_atomic_load((const u64*)(ws + off2), __ATOMIC_RELAXED, __HIP_MEMORY_SCOPE_AGENT);
    if (__all(v1 == tag && v2 == tag)) break;
    __builtin_amdgcn_s_sleep(1);
  }
  asm volatile("" ::: "memory");
}
DEV float wave_max32(float v) {
#pragma unroll
  for (int m = 16; m >= 1; m >>= 1) v = fmaxf(v, __builtin_bit_cast(float, __builtin_amdgcn_ds_bpermute(((threadIdx.x & 63) ^ m) << 2, __builtin_bit_cast(int, v))));
  return v;
}
DEV void publish(char* ws, unsigned off, u64 tag, int lane) {
  asm volatile("s_waitcnt vmcnt(0)" ::: "memory");
  if (lane == 0) __hip_atomic_store((u64*)(ws + off), tag, __ATOMIC_RELAXED, __HIP_MEMORY_SCOPE_AGENT);
}
DEV void lds_signal(LDSP char* sm, int idx, int lane) {
  asm volatile("s_waitcnt lgkmcnt(0)" ::: "memory");
  if (lane == 0) __hip_atomic_fetch_add((LDSP unsigned*)(sm + L_CNT + idx * 4), 1u, __ATOMIC_RELAXED, __HIP_MEMORY_SCOPE_WORKGROUP);
}
DEV void lds_wait(LDSP char* sm, int idx, unsigned n) {
  while (__builtin_amdgcn_readfirstlane(__hip_atomic_load((LDSP unsigned*)(sm + L_CNT + idx * 4), __ATOMIC_RELAXED, __HIP_MEMORY_SCOPE_WORKGROUP)) < n)
    __builtin_amdgcn_s_sleep(1);
  asm volatile("" ::: "memory");
}

template <bool B> struct BoolC { static constexpr bool value = B; };
struct Params {
  const char *x, *wq, *wk, *wv, *wo, *wg, *wu, *wd, *gw;
  char *out, *ws;
};

__global__ __launch_bounds__(512, 2) void fused_kernel(Params P) {
  __shared__ __attribute__((aligned(1024))) char smem_[LDS_BYTES];
  LDSP char* sm = (LDSP char*)smem_;
  char* ws = P.ws;
  const unsigned sbase = (unsigned)(size_t)sm;
  const int lane = threadIdx.x & 63;
  const int wave = __builtin_amdgcn_readfirstlane(threadIdx.x >> 6);
  const int bid = blockIdx.x;
  const int xcd = bid & 7, ii = bid >> 3;
  const int tb = (xcd / (NTB / 2)) * NTB + (ii % NTB), j = (xcd % (NTB / 2)) * (32 / NTB) + ii / NTB;
  const int r = lane & 31, h = lane >> 5;
  const rsrc_t rsW = mkrs(ws, WS_END);
  STAMP(0); STAMPC(14);
  u64 tag = 0, tagraw = 0;
  auto load_x = [&]() {
    const rsrc_t rsX = mkrs(P.x, 2048 * 896);
#pragma unroll
    for (int i = 0; i < 14; i++) {
      const int S = (wave * 14 + i) * 64 + lane;
      const int rr = S / 56, s = S - rr * 56;
      const int c = (s & ~7) | ((s & 7) ^ ((rr >> 1) & 7));
      __builtin_amdgcn_raw_ptr_buffer_load_lds(rsX, (LDSP void*)(sm + L_X + (wave * 14 + i) * 1024), 16, (tb * 128 + rr) * 896 + c * 16, 0, 0, 0);
    }
    tagraw = __hip_atomic_load((const u64*)(ws + WS_EPOCH), __ATOMIC_RELAXED, __HIP_MEMORY_SCOPE_AGENT);
    SB();
  };
  if (threadIdx.x < 8) *(LDSP unsigned*)(sm + L_CNT + threadIdx.x * 4) = 0;
  unsigned xa[4];
#pragma unroll
  for (int u = 0; u < 4; u++) xa[u] = r * 896 + (((2 * u + h) ^ ((r >> 1) & 7)) << 4);
  const unsigned tro = (8 * (lane >> 5) + ((lane & 15) >> 2)) * 64 + 32 * ((lane >> 4) & 1) + 8 * (lane & 3);

  if (wave < 4) {
    const int wa = wave;
    {
      const rsrc_t rsQ = mkrs(P.wq, 448 * 896), rsG = mkrs(P.gw, 896);
      load_x();
      const int u = lane & 7;
      u32x2 v[14];
      const unsigned qo = u < 7 ? (wa * 8 + (lane >> 3)) * 896 + 56 * j + 8 * u : OOB;
#pragma unroll
      for (int it = 0; it < 14; it++) v[it] = __builtin_amdgcn_raw_buffer_load_b64(rsQ, qo + it * 32 * 896, 0, 0);
      const int tid = wa * 64 + lane;
      const unsigned g0 = __builtin_amdgcn_raw_buffer_load_b16(rsG, tid * 2, 0, 0);
      const unsigned g1 = __builtin_amdgcn_raw_buffer_load_b16(rsG, (tid + 256) * 2, 0, 0);
      SB();
      if (u < 7) {
#pragma unroll
        for (int it = 0; it < 14; it++) *(LDSP u32x2*)(sm + L_WQ + (it * 256 + wa * 64 + lane) * 8) = v[it];
      }
      *(LDSP u32x2*)(sm + L_WQ + tid * 64 + 56) = u32x2{g0, 0u};
      if (tid < 192) *(LDSP u32x2*)(sm + L_WQ + (tid + 256) * 64 + 56) = u32x2{g1, 0u};
    }
    asm volatile("s_waitcnt vmcnt(0) lgkmcnt(0)" ::: "memory");
    __builtin_amdgcn_s_barrier();
    asm volatile("" ::: "memory");
    tag = tagraw + 1;
    STAMP(1);
    float gval;
    {
      __builtin_amdgcn_s_setprio(2);
      f32x16 q = {};
      const unsigned wqb = opq(sbase + L_WQ + tro);
      unsigned xq[4];
#pragma unroll
      for (int u = 0; u < 4; u++) xq[u] = opq(sbase + L_X + wa * 28672 + xa[u]);
      constexpr int PQ = 4;
      bf16x8 wf[PQ]; u32x4 xf[PQ];
#pragma unroll
      for (int s = 0; s < PQ; s++) { wf[s] = trfrag(lp(wqb) + s * 1024); xf[s] = lds16(lp(xq[s & 3]) + (s >> 2) * 128); }
#pragma unroll
      for (int s = 0; s < 28; s++) {
        SB();
        q = mfma(wf[s % PQ], asb(xf[s % PQ]), q);
        SB();
        if (s + PQ < 28) { wf[s % PQ] = trfrag(lp(wqb) + (s + PQ) * 1024); xf[s % PQ] = lds16(lp(xq[(s + PQ) & 3]) + ((s + PQ) >> 2) * 128); }
      }
      SB();
      const float logit = h ? q[12] : 0.f;
      gval = 1.f / (1.f + __expf(-(logit + xor32(logit))));
      const float qs = 0.18898223650461362f * 1.4426950408889634f;
      float n2 = 0.f;
#pragma unroll
      for (int e = 0; e < 16; e++) {
        q[e] *= qs;
        if (e >= 12 && h) q[e] = 0.f;
        n2 += q[e] * q[e];
      }
      lds16w(sm + L_QEX + wa * 2048 + lane * 16, packf(q, 0));
      lds16w(sm + L_QEX + wa * 2048 + 1024 + lane * 16, packf(q, 1));
      n2 += xor32(n2);
      n2 = wave_max32(n2);
      if (lane == 0) *(LDSP float*)(sm + L_CNT + 32 + wa * 4) = n2;
      if (!h) *(LDSP float*)(sm + L_OMG + (wa * 32 + r) * 4) = 1.f - gval;
      lds_signal(sm, 0, lane);
    }
    STAMP(2);
    lds_wait(sm, 0, 4);
    __builtin_amdgcn_s_setprio(APRIO);
    u32x4 qf[4][2];
#pragma unroll
    for (int t = 0; t < 4; t++) { qf[t][0] = lds16(sm + L_QEX + t * 2048 + lane * 16); qf[t][1] = lds16(sm + L_QEX + t * 2048 + 1024 + lane * 16); }
    f32x4 qm = *(LDSP f32x4*)(sm + L_CNT + 32);
    const float qmax2 = fmaxf(fmaxf(qm[0], qm[1]), fmaxf(qm[2], qm[3]));
    STAMP(6);
    poll(ws, WS_FKV + (32 * wa + (lane & 31)) * FSTR, lane < 32, tag);
    STAMP(3); STAMPC(12);
    const unsigned kvb = WS_KV + 16 * wa * 4096 + lane * 16;
    u32x4 K[4][2], V[4][2];
    const float kmx = __builtin_bit_cast(float, __builtin_amdgcn_raw_buffer_load_b32(rsW, WS_KV + (16 * wa + (lane & 15)) * 4096 + 1024 + 32 * 16 + 12, 0, AUX_SC1) << 16);
#pragma unroll
    for (int a = 0; a < 4; a++) {
      K[a][0] = ld16c(rsW, kvb + a * 4096); K[a][1] = ld16c(rsW, kvb + a * 4096 + 1024);
      V[a][0] = ld16c(rsW, kvb + a * 4096 + 2048); V[a][1] = ld16c(rsW, kvb + a * 4096 + 3072);
    }
    const unsigned tch = __builtin_amdgcn_raw_buffer_load_b32(rsW, WS_KV + 16 * wa * 4096 + (ii & 7) * 8192 + lane * 128, 0, AUX_SC1);
    const float kmax2 = wave_max(kmx);
    float cref[4] = {0.f, 0.f, 0.f, 0.f};
    if (!(qmax2 * kmax2 <= 3600.f)) {
      float mx[4] = {-3.0e38f, -3.0e38f, -3.0e38f, -3.0e38f};
#pragma unroll 1
      for (int kt = 0; kt < 16; kt++) {
        const u32x4 k0 = ld16c(rsW, kvb + kt * 4096), k1 = ld16c(rsW, kvb + kt * 4096 + 1024);
#pragma unroll
        for (int t = 0; t < 4; t++) {
          f32x16 s = {};
          s = mfma(asb(k0), asb(qf[t][0]), s);
          s = mfma(asb(k1), asb(qf[t][1]), s);
#pragma unroll
          for (int e = 0; e < 16; e++) mx[t] = fmaxf(mx[t], s[e]);
        }
      }
#pragma unroll
      for (int t = 0; t < 4; t++) {
        const float c = fmaxf(mx[t], xor32(mx[t]));
        const float chi = (float)(__bf16)c, clo = (float)(__bf16)(c - chi);
        cref[t] = chi + clo;
        if (h) qf[t][1][2] = pk2(-chi, -clo);
      }
    }
    if (!h) {
#pragma unroll
      for (int t = 0; t < 4; t++) *(LDSP float*)(sm + L_FB + (wa * 4 + t) * 128 + r * 4) = cref[t];
    }
    f32x16 o[4] = {};
    {
      const f32x16 zero = {};
      f32x16 S0, S1;
      u32x4 P0, PA1, PB1 = {0u, 0u, 0u, 0u};
      S0 = mfma(asb(K[0][0]), asb(qf[0][0]), zero);
      S0 = mfma(asb(K[0][1]), asb(qf[0][1]), S0);
      asm volatile("" ::"v"(tch));
#define UNIT(Sc, Sn, Kn, tn, Vp, Pp, tp, Vc, Pc, tc)                                                     \
      SB(); Sn = mfma(asb(Kn[0]), asb(qf[tn][0]), zero); SB();                                            \
      EXP4(Sc, 0); P0[0] = pk2(Sc[0], Sc[1]); P0[1] = pk2(Sc[2], Sc[3]);                                  \
      SB(); Sn = mfma(asb(Kn[1]), asb(qf[tn][1]), Sn); SB();                                              \
      EXP4(Sc, 4); P0[2] = pk2(Sc[4], Sc[5]); P0[3] = pk2(Sc[6], Sc[7]);                                  \
      SB(); o[tp] = mfma(asb(Vp[1]), asb(Pp), o[tp]); SB();                                               \
      EXP4(Sc, 8); Pc[0] = pk2(Sc[8], Sc[9]); Pc[1] = pk2(Sc[10], Sc[11]);                                \
      SB(); o[tc] = mfma(asb(Vc[0]), asb(P0), o[tc]); SB();                                               \
      EXP4(Sc, 12); Pc[2] = pk2(Sc[12], Sc[13]); Pc[3] = pk2(Sc[14], Sc[15]);
#define EXP4(S, b) S[b] = __builtin_amdgcn_exp2f(S[b]); S[b + 1] = __builtin_amdgcn_exp2f(S[b + 1]); S[b + 2] = __builtin_amdgcn_exp2f(S[b + 2]); S[b + 3] = __builtin_amdgcn_exp2f(S[b + 3]);
#define TILE(a, am, ap)                                                                                   \
        UNIT(S0, S1, K[a], 1, V[am], PB1, 3, V[a], PA1, 0)                                                \
        SB(); { const unsigned vo = (4 * it + a + 3 < 16) ? kvb + (4 * it + a + 3) * 4096 + 2048 : OOB;   \
                V[am][0] = ld16c(rsW, vo); V[am][1] = ld16c(rsW, vo + 1024); }                            \
        UNIT(S1, S0, K[a], 2, V[a], PA1, 0, V[a], PB1, 1)                                                 \
        UNIT(S0, S1, K[a], 3, V[a], PB1, 1, V[a], PA1, 2)                                                 \
        SB(); { const unsigned ko = (4 * it + a + 4 < 16) ? kvb + (4 * it + a + 4) * 4096 : OOB;          \
                K[a][0] = ld16c(rsW, ko); K[a][1] = ld16c(rsW, ko + 1024); }                              \
        UNIT(S1, S0, K[ap], 0, V[a], PA1, 2, V[a], PB1, 3)
#pragma unroll 1
      for (int it = 0; it < 4; it++) {
        TILE(0, 3, 1)
        TILE(1, 0, 2)
        TILE(2, 1, 3)
        TILE(3, 2, 0)
      }
      SB();
      o[3] = mfma(asb(V[3][1]), asb(PB1), o[3]);
    }
    STAMP(4); STAMPC(13);
    lds_wait(sm, 1, 4);
#pragma unroll
    for (int t = 0; t < 4; t++) {
      if (t != wa) {
#pragma unroll
        for (int qd = 0; qd < 4; qd++) {
          f32x4 v = {o[t][4 * qd], o[t][4 * qd + 1], o[t][4 * qd + 2], o[t][4 * qd + 3]};
          *(LDSP f32x4*)(sm + L_REDA + (wa * 4 + t) * 4096 + qd * 1024 + lane * 16) = v;
        }
      }
    }
    lds_signal(sm, 2, lane);
    f32x16 of = wa == 0 ? o[0] : wa == 1 ? o[1] : wa == 2 ? o[2] : o[3];
    lds_wait(sm, 2, 4);
    {
      float cw[4];
#pragma unroll
      for (int w = 0; w < 4; w++) cw[w] = *(LDSP float*)(sm + L_FB + (w * 4 + wa) * 128 + r * 4);
      const float cm = fmaxf(fmaxf(cw[0], cw[1]), fmaxf(cw[2], cw[3]));
      const float cown = wa == 0 ? cw[0] : wa == 1 ? cw[1] : wa == 2 ? cw[2] : cw[3];
      const float fo = __builtin_amdgcn_exp2f(cown - cm);
#pragma unroll
      for (int e = 0; e < 16; e++) of[e] *= fo;
#pragma unroll
      for (int w = 0; w < 4; w++) {
        if (w != wa) {
          const float f = __builtin_amdgcn_exp2f(cw[w] - cm);
#pragma unroll
          for (int qd = 0; qd < 4; qd++) {
            f32x4 v = *(LDSP f32x4*)(sm + L_REDA + (w * 4 + wa) * 4096 + qd * 1024 + lane * 16);
            of[4 * qd] += f * v[0]; of[4 * qd + 1] += f * v[1]; of[4 * qd + 2] += f * v[2]; of[4 * qd + 3] += f * v[3];
          }
        }
      }
    }
    {
      const float lh = h ? of[12] : 0.f;
      const float sc = gval / (lh + xor32(lh));
#pragma unroll
      for (int e = 0; e < 16; e++) { of[e] *= sc; if (e >= 12 && h) of[e] = 0.f; }
      const int T = 4 * tb + wa;
      st16c(rsW, WS_O + (T * 32 + 2 * j) * 1024 + lane * 16, packf(of, 0));
      st16c(rsW, WS_O + (T * 32 + 2 * j + 1) * 1024 + lane * 16, packf(of, 1));
      publish(ws, WS_FO + (T * 16 + j) * FSTR, tag, lane);
    }
    STAMP(5); STAMPC(15);
    if (bid == 0 && wa == 0) {
      for (int i = 0; i < 16; i++) poll(ws, WS_FO + (i * 64 + lane) * FSTR, true, tag);
      if (lane == 0) __hip_atomic_store((u64*)(ws + WS_EPOCH), tag, __ATOMIC_RELAXED, __HIP_MEMORY_SCOPE_AGENT);
    }
  } else {
    const int wb = wave - 4;
    LDSP char* slot = sm + L_BS + wb * 2048;
    if (j < 14) {
      constexpr int PD = 6;
      const rsrc_t rsG = mkrs(P.wg, 448 * 3584), rsU = mkrs(P.wu, 448 * 3584);
      const unsigned n0 = 128 * j + 32 * wb;
      const unsigned wo = (lane >> 2) * 3584 + (lane & 3) * 16 + n0 * 2;
      load_x();
      unsigned tw;
      {
        constexpr int CNT = 448 / NTB;
        const int li = ((ii % NTB) * 4 + wb) * CNT + lane;
        tw = __builtin_amdgcn_raw_buffer_load_b32((li & 2) ? rsU : rsG, lane < CNT ? (li >> 2) * 3584 + 256 * j + (li & 1) * 128 : OOB, 0, 0);
      }
      SB();
      u32x4 pre[PD][2];
#pragma unroll
      for (int s = 0; s < PD; s++) { pre[s][0] = ld16(rsG, wo + s * 57344); pre[s][1] = ld16(rsU, wo + s * 57344); }
      asm volatile("s_waitcnt vmcnt(13)" ::: "memory");
      __builtin_amdgcn_s_barrier();
      asm volatile("" ::: "memory");
      tag = tagraw + 1;
      STAMP(1);
      __builtin_amdgcn_s_setprio(HPRIO);
      f32x16 ag[4] = {}, au[4] = {};
      bf16x8 wf[2][2]; u32x4 xf[2][4];
      lds16w(slot + lane * 16, pre[0][0]); lds16w(slot + 1024 + lane * 16, pre[0][1]);
      asm volatile("" ::"v"(tw));
      pre[0][0] = ld16(rsG, wo + PD * 57344); pre[0][1] = ld16(rsU, wo + PD * 57344);
      wf[0][0] = trfrag(slot + tro); wf[0][1] = trfrag(slot + 1024 + tro);
#pragma unroll
      for (int t = 0; t < 4; t++) xf[0][t] = lds16(sm + L_X + t * 28672 + xa[0]);
#pragma unroll
      for (int s = 0; s < 28; s++) {
        const int c = s & 1, n = c ^ 1, s1 = s + 1;
        SB();
        ag[0] = mfma(wf[c][0], asb(xf[c][0]), ag[0]); au[0] = mfma(wf[c][1], asb(xf[c][0]), au[0]);
        SB();
        if (s1 < 28) {
          lds16w(slot + lane * 16, pre[s1 % PD][0]); lds16w(slot + 1024 + lane * 16, pre[s1 % PD][1]);
          if (s1 + PD < 28) { pre[s1 % PD][0] = ld16(rsG, wo + (s1 + PD) * 57344); pre[s1 % PD][1] = ld16(rsU, wo + (s1 + PD) * 57344); }
        }
        SB();
        ag[1] = mfma(wf[c][0], asb(xf[c][1]), ag[1]); au[1] = mfma(wf[c][1], asb(xf[c][1]), au[1]);
        SB();
        if (s1 < 28) {
          wf[n][0] = trfrag(slot + tro); wf[n][1] = trfrag(slot + 1024 + tro);
          xf[n][0] = lds16(sm + L_X + xa[s1 & 3] + (s1 >> 2) * 128);
        }
        SB();
        ag[2] = mfma(wf[c][0], asb(xf[c][2]), ag[2]); au[2] = mfma(wf[c][1], asb(xf[c][2]), au[2]);
        SB();
        if (s1 < 28) {
#pragma unroll
          for (int t = 1; t < 4; t++) xf[n][t] = lds16(sm + L_X + t * 28672 + xa[s1 & 3] + (s1 >> 2) * 128);
        }
        SB();
        ag[3] = mfma(wf[c][0], asb(xf[c][3]), ag[3]); au[3] = mfma(wf[c][1], asb(xf[c][3]), au[3]);
      }
      SB();
      lds_signal(sm, 1, lane);
      STAMP(2);
      __builtin_amdgcn_s_setprio(2);
      lds_wait(sm, 0, 4);
#pragma unroll
      for (int t = 0; t < 4; t++) {
        const float om = *(LDSP float*)(sm + L_OMG + (t * 32 + r) * 4);
#pragma unroll
        for (int e = 0; e < 16; e++) ag[t][e] *= au[t][e] * om;
        const unsigned hb = WS_H + ((4 * tb + t) * 112 + (n0 >> 4)) * 1024 + lane * 16;
        st16c(rsW, hb, packf(ag[t], 0));
        st16c(rsW, hb + 1024, packf(ag[t], 1));
      }
      publish(ws, WS_FH + (tb * 56 + 4 * j + wb) * FSTR, tag, lane);
      STAMP(3);
    } else {
      const bool isV = j == 15;
      const rsrc_t rsK = mkrs(isV ? P.wv : P.wk, 448 * 56);
      u32x4 v[28];
      if (!KVFIRST) load_x();
#pragma unroll
      for (int s = 0; s < 28; s++) v[s] = ld16(rsK, lane < 56 ? s * 896 + lane * 16 : OOB);
      SB();
      if (KVFIRST) { load_x(); asm volatile("s_waitcnt vmcnt(0)" ::: "memory"); } else asm volatile("s_waitcnt vmcnt(28)" ::: "memory");
      __builtin_amdgcn_s_barrier();
      asm volatile("" ::: "memory");
      tag = tagraw + 1;
      STAMP(1);
      __builtin_amdgcn_s_setprio(3);
      f32x16 a = {};
      const unsigned tro56 = (8 * (lane >> 5) + ((lane & 15) >> 2)) * 56 + 32 * ((lane >> 4) & 1) + 8 * (lane & 3);
      const unsigned slb = opq(sbase + L_BS + wb * 2048), slw = opq(slb + lane * 16), slr = opq(slb + tro56);
      unsigned xq[4];
#pragma unroll
      for (int u = 0; u < 4; u++) xq[u] = opq(sbase + L_X + wb * 28672 + xa[u]);
      auto kvgemm = [&](auto VT) {
        constexpr int PQ = 4;
        bf16x8 wf[PQ]; u32x4 xf[PQ];
        auto stage = [&](int s) {
          lds16w(lp(slw) + (s & 1) * 1024, v[s]);
          s16x4 t0 = __builtin_amdgcn_ds_read_tr16_b64_v4i16((LDSP s16x4*)(lp(slr) + (s & 1) * 1024));
          s16x4 t1 = __builtin_amdgcn_ds_read_tr16_b64_v4i16((LDSP s16x4*)(lp(slr) + (s & 1) * 1024 + 224));
          u32x2 ua = __builtin_bit_cast(u32x2, t0), ub = __builtin_bit_cast(u32x2, t1);
          wf[s % PQ] = asb(u32x4{ua[0], ua[1], ub[0], ub[1]});
          xf[s % PQ] = lds16(lp(xq[s & 3]) + (s >> 2) * 128);
        };
#pragma unroll
        for (int s = 0; s < PQ; s++) { SB(); stage(s); }
#pragma unroll
        for (int s = 0; s < 28; s++) {
          SB();
          if (VT.value) a = mfma(asb(xf[s % PQ]), wf[s % PQ], a); else a = mfma(wf[s % PQ], asb(xf[s % PQ]), a);
          SB();
          if (s + PQ < 28) stage(s + PQ);
        }
        SB();
      };
      if (isV) kvgemm(BoolC<true>{}); else kvgemm(BoolC<false>{});
      lds_signal(sm, 1, lane);
      STAMP(2);
      const int T = 4 * tb + wb;
      if (!isV) {
        float n2 = 0.f;
#pragma unroll
        for (int e = 0; e < 16; e++) {
          if (e >= 12 && h) a[e] = 0.f;
          n2 += a[e] * a[e];
          if ((e == 12 || e == 13) && h) a[e] = 1.f;
        }
        n2 += xor32(n2);
        n2 = wave_max(n2);
        if (h) a[14] = fminf(n2 * 1.0078125f, 3.0e38f);
      } else {
#pragma unroll
        for (int e = 0; e < 16; e++) a[e] = r < 28 ? a[e] : (r == 28 ? 1.f : 0.f);
      }
      const unsigned kb = WS_KV + T * 4096 + (isV ? 2048 : 0) + lane * 16;
      st16c(rsW, kb, packf(a, 0));
      st16c(rsW, kb + 1024, packf(a, 1));
      STAMP(8);
      publish(ws, WS_FKV + (T * 2 + (isV ? 1 : 0)) * FSTR, tag, lane);
      STAMP(3);
      const int pw = (tb * 2 + (j - 14)) * 4 + wb;
      if (pw < 126) {
        const int ir = lane >> 2, kk = (ir & 3) | ((ir & 4) << 1) | ((ir & 8) >> 1);
        u32x4 w3[16];
#pragma unroll
        for (int t = 0; t < 16; t++) {
          const int f = 16 * pw + t, ks = f / 14, nt = f - 14 * ks;
          const bool isd = ks < 112;
          const int ko = ks - 112, dim = 16 * (ko & 1) + kk;
          const unsigned row = isd ? 16 * ks + kk : 28 * (ko >> 1) + dim;
          w3[t] = ld16(mkrs(isd ? P.wd : P.wo, isd ? 1792 * 896 : 448 * 896), (!isd && dim >= 28) ? OOB : row * 896 + nt * 64 + (lane & 3) * 16);
        }
        SB();
#pragma unroll
        for (int t = 0; t < 16; t++) {
          lds16w(slot + (t & 1) * 1024 + lane * 16, w3[t]);
          st16c(rsW, WS_W3 + (16 * pw + t) * 1024 + lane * 16, __builtin_bit_cast(u32x4, trfrag(slot + (t & 1) * 1024 + tro)));
        }
        publish(ws, WS_FW + pw * FSTR, tag, lane);
      }
      STAMP(9);
    }
    __builtin_amdgcn_s_setprio(PPRIO);
    if (ii < 28) {
      constexpr int PD = P3PD;
      const int t64 = 4 * xcd + ii / 7, cb = ii % 7;
      const int T0 = 2 * t64;
      unsigned tc[3];
      {
        const int ks = lane < 28 ? 28 * wb + lane : 112 + 8 * wb + (lane - 28);
        poll(ws, WS_FW + ((ks * 14 + 2 * cb) >> 4) * FSTR, lane < 36, tag);
        const int li = (ii / 7) * 112 + lane, li2 = li + 64;
        tc[0] = __builtin_amdgcn_raw_buffer_load_b32(rsW, WS_W3 + ((28 * wb + (li >> 4)) * 14 + 2 * cb) * 1024 + (li & 15) * 128, 0, AUX_SC1);
        tc[1] = __builtin_amdgcn_raw_buffer_load_b32(rsW, lane < 48 ? WS_W3 + ((28 * wb + (li2 >> 4)) * 14 + 2 * cb) * 1024 + (li2 & 15) * 128 : OOB, 0, AUX_SC1);
        poll(ws, WS_FH + ((t64 >> 1) * 56 + 14 * wb + lane) * FSTR, lane < 14, tag);
        const int lh = cb * 64 + lane;
        tc[2] = __builtin_amdgcn_raw_buffer_load_b32(rsW, WS_H + ((2 * t64 + (lh >= 224)) * 112 + 28 * wb) * 1024 + (lh >= 224 ? lh - 224 : lh) * 128, 0, AUX_SC1);
      }
      STAMP(4);
      f32x16 acc[2][2] = {};
      const unsigned hb0 = WS_H + (T0 * 112 + 28 * wb) * 1024 + lane * 16, hb1 = hb0 + 112 * 1024;
      const unsigned wb0 = WS_W3 + (28 * wb * 14 + 2 * cb) * 1024 + lane * 16;
      u32x4 fr[PD][4];
#pragma unroll
      for (int s = 0; s < PD; s++) {
        fr[s][0] = ld16c(rsW, hb0 + s * 1024); fr[s][1] = ld16c(rsW, hb1 + s * 1024);
        fr[s][2] = ld16c(rsW, wb0 + s * 14336); fr[s][3] = ld16c(rsW, wb0 + s * 14336 + 1024);
      }
#pragma unroll
      for (int s = 0; s < 28; s++) {
        const int c = s % PD;
        SB();
        if (s == 1) asm volatile("" ::"v"(tc[0]), "v"(tc[1]), "v"(tc[2]));
        acc[0][0] = mfma(asb(fr[c][2]), asb(fr[c][0]), acc[0][0]);
        acc[0][1] = mfma(asb(fr[c][2]), asb(fr[c][1]), acc[0][1]);
        acc[1][0] = mfma(asb(fr[c][3]), asb(fr[c][0]), acc[1][0]);
        acc[1][1] = mfma(asb(fr[c][3]), asb(fr[c][1]), acc[1][1]);
        SB();
        if (s + PD < 28) {
          fr[c][0] = ld16c(rsW, hb0 + (s + PD) * 1024); fr[c][1] = ld16c(rsW, hb1 + (s + PD) * 1024);
          fr[c][2] = ld16c(rsW, wb0 + (s + PD) * 14336); fr[c][3] = ld16c(rsW, wb0 + (s + PD) * 14336 + 1024);
        }
      }
      SB();
      STAMP(5);
      const unsigned wo0 = WS_W3 + ((112 + 8 * wb) * 14 + 2 * cb) * 1024 + lane * 16;
      u32x4 wo_[8][2];
#pragma unroll
      for (int s = 0; s < 8; s++) { wo_[s][0] = ld16c(rsW, wo0 + s * 14336); wo_[s][1] = ld16c(rsW, wo0 + s * 14336 + 1024); }
      poll(ws, WS_FO + ((T0 + (lane >> 2)) * 16 + 4 * wb + (lane & 3)) * FSTR, lane < 8, tag);
      STAMP(6);
      const unsigned ob0 = WS_O + (T0 * 32 + 8 * wb) * 1024 + lane * 16, ob1 = ob0 + 32 * 1024;
      u32x4 of[8][2];
#pragma unroll
      for (int s = 0; s < 8; s++) { of[s][0] = ld16c(rsW, ob0 + s * 1024); of[s][1] = ld16c(rsW, ob1 + s * 1024); }
#pragma unroll
      for (int s = 0; s < 8; s++) {
        acc[0][0] = mfma(asb(wo_[s][0]), asb(of[s][0]), acc[0][0]);
        acc[0][1] = mfma(asb(wo_[s][0]), asb(of[s][1]), acc[0][1]);
        acc[1][0] = mfma(asb(wo_[s][1]), asb(of[s][0]), acc[1][0]);
        acc[1][1] = mfma(asb(wo_[s][1]), asb(of[s][1]), acc[1][1]);
      }
      lds_wait(sm, 0, 4);
      lds_wait(sm, 1, 4);
#pragma unroll
      for (int t = 0; t < 4; t++) {
        if (t != wb) {
#pragma unroll
          for (int qd = 0; qd < 4; qd++) {
            const f32x16& a = acc[t >> 1][t & 1];
            f32x4 v = {a[4 * qd], a[4 * qd + 1], a[4 * qd + 2], a[4 * qd + 3]};
            *(LDSP f32x4*)(sm + L_REDB + (wb * 4 + t) * 4096 + qd * 1024 + lane * 16) = v;
          }
        }
      }
      lds_signal(sm, 3, lane);
      f32x16 z = wb == 0 ? acc[0][0] : wb == 1 ? acc[0][1] : wb == 2 ? acc[1][0] : acc[1][1];
      lds_wait(sm, 3, 4);
#pragma unroll
      for (int w = 0; w < 4; w++) {
        if (w != wb) {
#pragma unroll
          for (int qd = 0; qd < 4; qd++) {
            f32x4 v = *(LDSP f32x4*)(sm + L_REDB + (w * 4 + wb) * 4096 + qd * 1024 + lane * 16);
            z[4 * qd] += v[0]; z[4 * qd + 1] += v[1]; z[4 * qd + 2] += v[2]; z[4 * qd + 3] += v[3];
          }
        }
      }
      const rsrc_t rsO = mkrs(P.out, 2048 * 896);
      const unsigned ob = ((T0 + (wb & 1)) * 32 + r) * 896 + (64 * cb + 32 * (wb >> 1) + 4 * h) * 2;
#pragma unroll
      for (int qd = 0; qd < 4; qd++) {
        u32x2 v = {pk2(z[4 * qd], z[4 * qd + 1]), pk2(z[4 * qd + 2], z[4 * qd + 3])};
        __builtin_amdgcn_raw_buffer_store_b64(v, rsO, ob + qd * 16, 0, 0);
      }
      STAMP(7);
    }
  }
  STAMPS_OUT();
}

extern "C" void kernel_launch(void* const* d_in, const int* in_sizes, int n_in, void* d_out, int out_size, void* d_ws, size_t ws_size, hipStream_t stream) {
  Params P;
  P.x = (const char*)d_in[0]; P.wq = (const char*)d_in[1]; P.wk = (const char*)d_in[2]; P.wv = (const char*)d_in[3]; P.wo = (const char*)d_in[4];
  P.wg = (const char*)d_in[6]; P.wu = (const char*)d_in[7]; P.wd = (const char*)d_in[8]; P.gw = (const char*)d_in[9];
  P.out = (char*)d_out; P.ws = (char*)d_ws;
  fused_kernel<<<256, 512, 0, stream>>>(P);
}
```

```cpp
#include <hip/hip_runtime.h>
#include <hip/hip_bf16.h>
#include <stdint.h>

typedef __bf16 bf16x8 __attribute__((ext_vector_type(8)));
typedef __bf16 bf16x2 __attribute__((ext_vector_type(2)));
typedef short s16x4 __attribute__((ext_vector_type(4)));
typedef float f32x16 __attribute__((ext_vector_type(16)));
typedef float f32x4 __attribute__((ext_vector_type(4)));
typedef unsigned u32x4 __attribute__((ext_vector_type(4)));
typedef unsigned u32x2 __attribute__((ext_vector_type(2)));
typedef unsigned long long u64;
typedef __amdgpu_buffer_rsrc_t rsrc_t;
#define LDSP __attribute__((address_space(3)))
#define DEV __device__ __forceinline__
#define SB() __builtin_amdgcn_sched_barrier(0)

constexpr unsigned WS_EPOCH = 0;
#define FSTR 64
#define HPRIO 1
#define PPRIO 0
#define APRIO 0
#define KVFIRST 0
#define P3PD 6
#define SLP 4
constexpr unsigned WS_FLAGS = 11829248;
constexpr unsigned WS_FKV = WS_FLAGS;
constexpr unsigned WS_FH = WS_FKV + 128 * FSTR;
constexpr unsigned WS_FO = WS_FH + 896 * FSTR;
constexpr unsigned WS_FW = WS_FO + 1024 * FSTR;
constexpr unsigned WS_KV = 65536;
constexpr unsigned WS_O = 327680;
constexpr unsigned WS_W3 = 2424832;
constexpr unsigned WS_H = 4489216;
constexpr unsigned WS_END = WS_FLAGS;
constexpr unsigned PROF_OFF = 12582912;
constexpr unsigned L_X = 0;
constexpr unsigned L_WQ = 114688;
constexpr unsigned L_QEX = 143360;
constexpr unsigned L_BS = 151552;
constexpr unsigned L_CNT = 159744;
constexpr unsigned L_FB = 160000 - 160000 % 256 + 256;
constexpr unsigned L_OMG = L_FB + 2048;
constexpr unsigned L_REDA = 0;
constexpr unsigned L_REDB = 65536;
constexpr unsigned LDS_BYTES = 163840;
constexpr int AUX_SC1 = 16;
#define NTB 8
constexpr unsigned OOB = 0x80000000u;

#define STAMP(e)
#define STAMPC(e)
#define STAMPS_OUT()

DEV unsigned opq(unsigned v) { asm volatile("" : "+v"(v)); return v; }
DEV LDSP char* lp(unsigned a) { return (LDSP char*)(size_t)a; }
DEV rsrc_t mkrs(const void* p, unsigned bytes) { return __builtin_amdgcn_make_buffer_rsrc((void*)p, (short)0, (int)bytes, 0x00020000); }
DEV u32x4 ld16(rsrc_t r, unsigned off) { return __builtin_amdgcn_raw_buffer_load_b128(r, off, 0, 0); }
DEV u32x4 ld16c(rsrc_t r, unsigned off) { return __builtin_amdgcn_raw_buffer_load_b128(r, off, 0, AUX_SC1); }
DEV void st16c(rsrc_t r, unsigned off, u32x4 v) { __builtin_amdgcn_raw_buffer_store_b128(v, r, off, 0, AUX_SC1); }
DEV unsigned pk2(float a, float b) { bf16x2 v = {(__bf16)a, (__bf16)b}; return __builtin_bit_cast(unsigned, v); }
DEV bf16x8 asb(u32x4 v) { return __builtin_bit_cast(bf16x8, v); }
DEV f32x16 mfma(bf16x8 a, bf16x8 b, f32x16 c) { return __builtin_amdgcn_mfma_f32_32x32x16_bf16(a, b, c, 0, 0, 0); }
DEV u32x4 packf(const f32x16& a, int s2) {
  u32x4 r;
  r[0] = pk2(a[8 * s2 + 0], a[8 * s2 + 1]); r[1] = pk2(a[8 * s2 + 2], a[8 * s2 + 3]);
  r[2] = pk2(a[8 * s2 + 4], a[8 * s2 + 5]); r[3] = pk2(a[8 * s2 + 6], a[8 * s2 + 7]);
  return r;
}
DEV u32x4 lds16(LDSP char* p) { return *(LDSP u32x4*)p; }
DEV void lds16w(LDSP char* p, u32x4 v) { *(LDSP u32x4*)p = v; }
DEV bf16x8 trfrag(LDSP char* p) {
  s16x4 a = __builtin_amdgcn_ds_read_tr16_b64_v4i16((LDSP s16x4*)p);
  s16x4 b = __builtin_amdgcn_ds_read_tr16_b64_v4i16((LDSP s16x4*)(p + 256));
  u32x2 ua = __builtin_bit_cast(u32x2, a), ub = __builtin_bit_cast(u32x2, b);
  u32x4 r = {ua[0], ua[1], ub[0], ub[1]};
  return asb(r);
}
DEV float xor32(float v) { return __builtin_bit_cast(float, __builtin_amdgcn_ds_bpermute(((threadIdx.x & 63) ^ 32) << 2, __builtin_bit_cast(int, v))); }
DEV float wave_max(float v) {
#pragma unroll
  for (int m = 32; m >= 1; m >>= 1) v = fmaxf(v, __builtin_bit_cast(float, __builtin_amdgcn_ds_bpermute(((threadIdx.x & 63) ^ m) << 2, __builtin_bit_cast(int, v))));
  return v;
}
DEV void poll(const char* ws, unsigned off, bool active, u64 tag) {
  for (;;) {
    u64 v = tag;
    if (active) v = __hip_atomic_load((const u64*)(ws + off), __ATOMIC_RELAXED, __HIP_MEMORY_SCOPE_AGENT);
    if (__all(v == tag)) break;
    __builtin_amdgcn_s_sleep(SLP);
  }
  asm volatile("" ::: "memory");
}
DEV void poll2(const char* ws, unsigned off1, bool act1, unsigned off2, bool act2, u64 tag) {
  for (;;) {
    u64 v1 = tag, v2 = tag;
    if (act1) v1 = __hip_atomic_load((const u64*)(ws + off1), __ATOMIC_RELAXED, __HIP_MEMORY_SCOPE_AGENT);
    if (act2) v2 = __hip_atomic_load((const u64*)(ws + off2), __ATOMIC_RELAXED, __HIP_MEMORY_SCOPE_AGENT);
    if (__all(v1 == tag && v2 == tag)) break;
    __builtin_amdgcn_s_sleep(1);
  }
  asm volatile("" ::: "memory");
}
DEV float wave_max32(float v) {
#pragma unroll
  for (int m = 16; m >= 1; m >>= 1) v = fmaxf(v, __builtin_bit_cast(float, __builtin_amdgcn_ds_bpermute(((threadIdx.x & 63) ^ m) << 2, __builtin_bit_cast(int, v))));
  return v;
}
DEV void publish(char* ws, unsigned off, u64 tag, int lane) {
  asm volatile("s_waitcnt vmcnt(0)" ::: "memory");
  if (lane == 0) __hip_atomic_store((u64*)(ws + off), tag, __ATOMIC_RELAXED, __HIP_MEMORY_SCOPE_AGENT);
}
DEV void lds_signal(LDSP char* sm, int idx, int lane) {
  asm volatile("s_waitcnt lgkmcnt(0)" ::: "memory");
  if (lane == 0) __hip_atomic_fetch_add((LDSP unsigned*)(sm + L_CNT + idx * 4), 1u, __ATOMIC_RELAXED, __HIP_MEMORY_SCOPE_WORKGROUP);
}
DEV void lds_wait(LDSP char* sm, int idx, unsigned n) {
  while (__builtin_amdgcn_readfirstlane(__hip_atomic_load((LDSP unsigned*)(sm + L_CNT + idx * 4), __ATOMIC_RELAXED, __HIP_MEMORY_SCOPE_WORKGROUP)) < n)
    __builtin_amdgcn_s_sleep(1);
  asm volatile("" ::: "memory");
}

template <bool B> struct BoolC { static constexpr bool value = B; };
struct Params {
  const char *x, *wq, *wk, *wv, *wo, *wg, *wu, *wd, *gw;
  char *out, *ws;
};

__global__ __launch_bounds__(512, 2) void fused_kernel(Params P) {
  __shared__ __attribute__((aligned(1024))) char smem_[LDS_BYTES];
  LDSP char* sm = (LDSP char*)smem_;
  char* ws = P.ws;
  const unsigned sbase = (unsigned)(size_t)sm;
  const int lane = threadIdx.x & 63;
  const int wave = __builtin_amdgcn_readfirstlane(threadIdx.x >> 6);
  const int bid = blockIdx.x;
  const int xcd = bid & 7, ii = bid >> 3;
  const int tb = (xcd / (NTB / 2)) * NTB + (ii % NTB), j = (xcd % (NTB / 2)) * (32 / NTB) + ii / NTB;
  const int r = lane & 31, h = lane >> 5;
  const rsrc_t rsW = mkrs(ws, WS_END);
  STAMP(0); STAMPC(14);
  u64 tag = 0, tagraw = 0;
  auto load_x = [&]() {
    const rsrc_t rsX = mkrs(P.x, 2048 * 896);
#pragma unroll
    for (int i = 0; i < 14; i++) {
      const int S = (wave * 14 + i) * 64 + lane;
      const int rr = S / 56, s = S - rr * 56;
      const int c = (s & ~7) | ((s & 7) ^ ((rr >> 1) & 7));
      __builtin_amdgcn_raw_ptr_buffer_load_lds(rsX, (LDSP void*)(sm + L_X + (wave * 14 + i) * 1024), 16, (tb * 128 + rr) * 896 + c * 16, 0, 0, 0);
    }
    tagraw = __hip_atomic_load((const u64*)(ws + WS_EPOCH), __ATOMIC_RELAXED, __HIP_MEMORY_SCOPE_AGENT);
    SB();
  };
  if (threadIdx.x < 8) *(LDSP unsigned*)(sm + L_CNT + threadIdx.x * 4) = 0;
  unsigned xa[4];
#pragma unroll
  for (int u = 0; u < 4; u++) xa[u] = r * 896 + (((2 * u + h) ^ ((r >> 1) & 7)) << 4);
  const unsigned tro = (8 * (lane >> 5) + ((lane & 15) >> 2)) * 64 + 32 * ((lane >> 4) & 1) + 8 * (lane & 3);

  if (wave < 4) {
    const int wa = wave;
    const int pw = bid * 4 + wa;
    u32x4 w3[2];
    {
      const rsrc_t rsQ = mkrs(P.wq, 448 * 896), rsG = mkrs(P.gw, 896);
      load_x();
      const int u = lane & 7;
      u32x2 v[14];
      const unsigned qo = u < 7 ? (wa * 8 + (lane >> 3)) * 896 + 56 * j + 8 * u : OOB;
#pragma unroll
      for (int it = 0; it < 14; it++) v[it] = __builtin_amdgcn_raw_buffer_load_b64(rsQ, qo + it * 32 * 896, 0, 0);
      const int tid = wa * 64 + lane;
      const unsigned g0 = __builtin_amdgcn_raw_buffer_load_b16(rsG, tid * 2, 0, 0);
      const unsigned g1 = __builtin_amdgcn_raw_buffer_load_b16(rsG, (tid + 256) * 2, 0, 0);
      SB();
      {
        const int ir = lane >> 2, kk = (ir & 3) | ((ir & 4) << 1) | ((ir & 8) >> 1);
        const int ks = pw / 7, cb = pw - 7 * ks, ko = ks - 112;
        const bool isd = ks < 112;
        const int row0 = isd ? 16 * ks : 28 * (ko >> 1) + 16 * (ko & 1);
        const unsigned vo = (pw >= 1008 || (!isd && (ko & 1) && kk >= 12)) ? OOB : kk * 896 + (lane & 3) * 16;
        const rsrc_t rs3 = mkrs(isd ? P.wd : P.wo, isd ? 1792 * 896 : 448 * 896);
        w3[0] = __builtin_amdgcn_raw_buffer_load_b128(rs3, vo, pw >= 1008 ? 0 : row0 * 896 + cb * 128, 0);
        w3[1] = __builtin_amdgcn_raw_buffer_load_b128(rs3, vo, pw >= 1008 ? 0 : row0 * 896 + cb * 128 + 64, 0);
      }
      SB();
      if (u < 7) {
#pragma unroll
        for (int it = 0; it < 14; it++) *(LDSP u32x2*)(sm + L_WQ + (it * 256 + wa * 64 + lane) * 8) = v[it];
      }
      *(LDSP u32x2*)(sm + L_WQ + tid * 64 + 56) = u32x2{g0, 0u};
      if (tid < 192) *(LDSP u32x2*)(sm + L_WQ + (tid + 256) * 64 + 56) = u32x2{g1, 0u};
    }
    asm volatile("s_waitcnt vmcnt(2) lgkmcnt(0)" ::: "memory");
    __builtin_amdgcn_s_barrier();
    asm volatile("" ::: "memory");
    tag = tagraw + 1;
    STAMP(1);
    if (pw < 1008) {
      LDSP char* slot = sm + L_QEX + wa * 2048;
      lds16w(slot + lane * 16, w3[0]); lds16w(slot + 1024 + lane * 16, w3[1]);
      const unsigned dst = WS_W3 + ((pw / 7) * 14 + 2 * (pw % 7)) * 1024 + lane * 16;
      st16c(rsW, dst, __builtin_bit_cast(u32x4, trfrag(slot + tro)));
      st16c(rsW, dst + 1024, __builtin_bit_cast(u32x4, trfrag(slot + 1024 + tro)));
    }
    float gval;
    {
      __builtin_amdgcn_s_setprio(2);
      f32x16 q = {};
      const unsigned wqb = opq(sbase + L_WQ + tro);
      unsigned xq[4];
#pragma unroll
      for (int u = 0; u < 4; u++) xq[u] = opq(sbase + L_X + wa * 28672 + xa[u]);
      constexpr int PQ = 4;
      bf16x8 wf[PQ]; u32x4 xf[PQ];
#pragma unroll
      for (int s = 0; s < PQ; s++) { wf[s] = trfrag(lp(wqb) + s * 1024); xf[s] = lds16(lp(xq[s & 3]) + (s >> 2) * 128); }
#pragma unroll
      for (int s = 0; s < 28; s++) {
        SB();
        q = mfma(wf[s % PQ], asb(xf[s % PQ]), q);
        SB();
        if (s + PQ < 28) { wf[s % PQ] = trfrag(lp(wqb) + (s + PQ) * 1024); xf[s % PQ] = lds16(lp(xq[(s + PQ) & 3]) + ((s + PQ) >> 2) * 128); }
      }
      SB();
      const float logit = h ? q[12] : 0.f;
      gval = 1.f / (1.f + __expf(-(logit + xor32(logit))));
      const float qs = 0.18898223650461362f * 1.4426950408889634f;
      float n2 = 0.f;
#pragma unroll
      for (int e = 0; e < 16; e++) {
        q[e] *= qs;
        if (e >= 12 && h) q[e] = 0.f;
        n2 += q[e] * q[e];
      }
      lds16w(sm + L_QEX + wa * 2048 + lane * 16, packf(q, 0));
      lds16w(sm + L_QEX + wa * 2048 + 1024 + lane * 16, packf(q, 1));
      n2 += xor32(n2);
      n2 = wave_max32(n2);
      if (lane == 0) *(LDSP float*)(sm + L_CNT + 32 + wa * 4) = n2;
      if (!h) *(LDSP float*)(sm + L_OMG + (wa * 32 + r) * 4) = 1.f - gval;
      lds_signal(sm, 0, lane);
    }
    STAMP(2);
    lds_wait(sm, 0, 4);
    __builtin_amdgcn_s_setprio(APRIO);
    u32x4 qf[4][2];
#pragma unroll
    for (int t = 0; t < 4; t++) { qf[t][0] = lds16(sm + L_QEX + t * 2048 + lane * 16); qf[t][1] = lds16(sm + L_QEX + t * 2048 + 1024 + lane * 16); }
    f32x4 qm = *(LDSP f32x4*)(sm + L_CNT + 32);
    const float qmax2 = fmaxf(fmaxf(qm[0], qm[1]), fmaxf(qm[2], qm[3]));
    STAMP(6);
    poll(ws, WS_FKV + (32 * wa + (lane & 31)) * FSTR, lane < 32, tag);
    if (pw < 1008) publish(ws, WS_FW + pw * FSTR, tag, lane);
    STAMP(3); STAMPC(12);
    const unsigned kvb = WS_KV + 16 * wa * 4096 + lane * 16;
    u32x4 K[4][2], V[4][2];
    const float kmx = __builtin_bit_cast(float, __builtin_amdgcn_raw_buffer_load_b32(rsW, WS_KV + (16 * wa + (lane & 15)) * 4096 + 1024 + 32 * 16 + 12, 0, AUX_SC1) << 16);
#pragma unroll
    for (int a = 0; a < 4; a++) {
      K[a][0] = ld16c(rsW, kvb + a * 4096); K[a][1] = ld16c(rsW, kvb + a * 4096 + 1024);
      V[a][0] = ld16c(rsW, kvb + a * 4096 + 2048); V[a][1] = ld16c(rsW, kvb + a * 4096 + 3072);
    }
    const unsigned tch = __builtin_amdgcn_raw_buffer_load_b32(rsW, WS_KV + 16 * wa * 4096 + (ii & 7) * 8192 + lane * 128, 0, AUX_SC1);
    const float kmax2 = wave_max(kmx);
    float cref[4] = {0.f, 0.f, 0.f, 0.f};
    if (!(qmax2 * kmax2 <= 3600.f)) {
      float mx[4] = {-3.0e38f, -3.0e38f, -3.0e38f, -3.0e38f};
#pragma unroll 1
      for (int kt = 0; kt < 16; kt++) {
        const u32x4 k0 = ld16c(rsW, kvb + kt * 4096), k1 = ld16c(rsW, kvb + kt * 4096 + 1024);
#pragma unroll
        for (int t = 0; t < 4; t++) {
          f32x16 s = {};
          s = mfma(asb(k0), asb(qf[t][0]), s);
          s = mfma(asb(k1), asb(qf[t][1]), s);
#pragma unroll
          for (int e = 0; e < 16; e++) mx[t] = fmaxf(mx[t], s[e]);
        }
      }
#pragma unroll
      for (int t = 0; t < 4; t++) {
        const float c = fmaxf(mx[t], xor32(mx[t]));
        const float chi = (float)(__bf16)c, clo = (float)(__bf16)(c - chi);
        cref[t] = chi + clo;
        if (h) qf[t][1][2] = pk2(-chi, -clo);
      }
    }
    if (!h) {
#pragma unroll
      for (int t = 0; t < 4; t++) *(LDSP float*)(sm + L_FB + (wa * 4 + t) * 128 + r * 4) = cref[t];
    }
    f32x16 o[4] = {};
    {
      const f32x16 zero = {};
      f32x16 S0, S1;
      u32x4 P0, PA1, PB1 = {0u, 0u, 0u, 0u};
      S0 = mfma(asb(K[0][0]), asb(qf[0][0]), zero);
      S0 = mfma(asb(K[0][1]), asb(qf[0][1]), S0);
      asm volatile("" ::"v"(tch));
#define UNIT(Sc, Sn, Kn, tn, Vp, Pp, tp, Vc, Pc, tc)                                                     \
      SB(); Sn = mfma(asb(Kn[0]), asb(qf[tn][0]), zero); SB();                                            \
      EXP4(Sc, 0); P0[0] = pk2(Sc[0], Sc[1]); P0[1] = pk2(Sc[2], Sc[3]);                                  \
      SB(); Sn = mfma(asb(Kn[1]), asb(qf[tn][1]), Sn); SB();                                              \
      EXP4(Sc, 4); P0[2] = pk2(Sc[4], Sc[5]); P0[3] = pk2(Sc[6], Sc[7]);                                  \
      SB(); o[tp] = mfma(asb(Vp[1]), asb(Pp), o[tp]); SB();                                               \
      EXP4(Sc, 8); Pc[0] = pk2(Sc[8], Sc[9]); Pc[1] = pk2(Sc[10], Sc[11]);                                \
      SB(); o[tc] = mfma(asb(Vc[0]), asb(P0), o[tc]); SB();                                               \
      EXP4(Sc, 12); Pc[2] = pk2(Sc[12], Sc[13]); Pc[3] = pk2(Sc[14], Sc[15]);
#define EXP4(S, b) S[b] = __builtin_amdgcn_exp2f(S[b]); S[b + 1] = __builtin_amdgcn_exp2f(S[b + 1]); S[b + 2] = __builtin_amdgcn_exp2f(S[b + 2]); S[b + 3] = __builtin_amdgcn_exp2f(S[b + 3]);
#define TILE(a, am, ap)                                                                                   \
        UNIT(S0, S1, K[a], 1, V[am], PB1, 3, V[a], PA1, 0)                                                \
        SB(); { const unsigned vo = (4 * it + a + 3 < 16) ? kvb + (4 * it + a + 3) * 4096 + 2048 : OOB;   \
                V[am][0] = ld16c(rsW, vo); V[am][1] = ld16c(rsW, vo + 1024); }                            \
        UNIT(S1, S0, K[a], 2, V[a], PA1, 0, V[a], PB1, 1)                                                 \
        UNIT(S0, S1, K[a], 3, V[a], PB1, 1, V[a], PA1, 2)                                                 \
        SB(); { const unsigned ko = (4 * it + a + 4 < 16) ? kvb + (4 * it + a + 4) * 4096 : OOB;          \
                K[a][0] = ld16c(rsW, ko); K[a][1] = ld16c(rsW, ko + 1024); }                              \
        UNIT(S1, S0, K[ap], 0, V[a], PA1, 2, V[a], PB1, 3)
#pragma unroll 1
      for (int it = 0; it < 4; it++) {
        TILE(0, 3, 1)
        TILE(1, 0, 2)
        TILE(2, 1, 3)
        TILE(3, 2, 0)
      }
      SB();
      o[3] = mfma(asb(V[3][1]), asb(PB1), o[3]);
    }
    STAMP(4); STAMPC(13);
    lds_wait(sm, 1, 4);
#pragma unroll
    for (int t = 0; t < 4; t++) {
      if (t != wa) {
#pragma unroll
        for (int qd = 0; qd < 4; qd++) {
          f32x4 v = {o[t][4 * qd], o[t][4 * qd + 1], o[t][4 * qd + 2], o[t][4 * qd + 3]};
          *(LDSP f32x4*)(sm + L_REDA + (wa * 4 + t) * 4096 + qd * 1024 + lane * 16) = v;
        }
      }
    }
    lds_signal(sm, 2, lane);
    f32x16 of = wa == 0 ? o[0] : wa == 1 ? o[1] : wa == 2 ? o[2] : o[3];
    lds_wait(sm, 2, 4);
    {
      float cw[4];
#pragma unroll
      for (int w = 0; w < 4; w++) cw[w] = *(LDSP float*)(sm + L_FB + (w * 4 + wa) * 128 + r * 4);
      const float cm = fmaxf(fmaxf(cw[0], cw[1]), fmaxf(cw[2], cw[3]));
      const float cown = wa == 0 ? cw[0] : wa == 1 ? cw[1] : wa == 2 ? cw[2] : cw[3];
      const float fo = __builtin_amdgcn_exp2f(cown - cm);
#pragma unroll
      for (int e = 0; e < 16; e++) of[e] *= fo;
#pragma unroll
      for (int w = 0; w < 4; w++) {
        if (w != wa) {
          const float f = __builtin_amdgcn_exp2f(cw[w] - cm);
#pragma unroll
          for (int qd = 0; qd < 4; qd++) {
            f32x4 v = *(LDSP f32x4*)(sm + L_REDA + (w * 4 + wa) * 4096 + qd * 1024 + lane * 16);
            of[4 * qd] += f * v[0]; of[4 * qd + 1] += f * v[1]; of[4 * qd + 2] += f * v[2]; of[4 * qd + 3] += f * v[3];
          }
        }
      }
    }
    {
      const float lh = h ? of[12] : 0.f;
      const float sc = gval / (lh + xor32(lh));
#pragma unroll
      for (int e = 0; e < 16; e++) { of[e] *= sc; if (e >= 12 && h) of[e] = 0.f; }
      const int T = 4 * tb + wa;
      st16c(rsW, WS_O + (T * 32 + 2 * j) * 1024 + lane * 16, packf(of, 0));
      st16c(rsW, WS_O + (T * 32 + 2 * j + 1) * 1024 + lane * 16, packf(of, 1));
      publish(ws, WS_FO + (T * 16 + j) * FSTR, tag, lane);
    }
    STAMP(5); STAMPC(15);
  } else {
    const int wb = wave - 4;
    LDSP char* slot = sm + L_BS + wb * 2048;
    if (j < 14) {
      constexpr int PD = 6;
      const rsrc_t rsG = mkrs(P.wg, 448 * 3584), rsU = mkrs(P.wu, 448 * 3584);
      const unsigned n0 = 128 * j + 32 * wb;
      const unsigned wo = (lane >> 2) * 3584 + (lane & 3) * 16 + n0 * 2;
      load_x();
      unsigned tw;
      {
        constexpr int CNT = 448 / NTB;
        const int li = ((ii % NTB) * 4 + wb) * CNT + lane;
        tw = __builtin_amdgcn_raw_buffer_load_b32((li & 2) ? rsU : rsG, lane < CNT ? (li >> 2) * 3584 + 256 * j + (li & 1) * 128 : OOB, 0, 0);
      }
      SB();
      u32x4 pre[PD][2];
#pragma unroll
      for (int s = 0; s < PD; s++) { pre[s][0] = ld16(rsG, wo + s * 57344); pre[s][1] = ld16(rsU, wo + s * 57344); }
      asm volatile("s_waitcnt vmcnt(13)" ::: "memory");
      __builtin_amdgcn_s_barrier();
      asm volatile("" ::: "memory");
      tag = tagraw + 1;
      STAMP(1);
      __builtin_amdgcn_s_setprio(HPRIO);
      f32x16 ag[4] = {}, au[4] = {};
      bf16x8 wf[2][2]; u32x4 xf[2][4];
      lds16w(slot + lane * 16, pre[0][0]); lds16w(slot + 1024 + lane * 16, pre[0][1]);
      asm volatile("" ::"v"(tw));
      pre[0][0] = ld16(rsG, wo + PD * 57344); pre[0][1] = ld16(rsU, wo + PD * 57344);
      wf[0][0] = trfrag(slot + tro); wf[0][1] = trfrag(slot + 1024 + tro);
#pragma unroll
      for (int t = 0; t < 4; t++) xf[0][t] = lds16(sm + L_X + t * 28672 + xa[0]);
#pragma unroll
      for (int s = 0; s < 28; s++) {
        const int c = s & 1, n = c ^ 1, s1 = s + 1;
        SB();
        ag[0] = mfma(wf[c][0], asb(xf[c][0]), ag[0]); au[0] = mfma(wf[c][1], asb(xf[c][0]), au[0]);
        SB();
        if (s1 < 28) {
          lds16w(slot + lane * 16, pre[s1 % PD][0]); lds16w(slot + 1024 + lane * 16, pre[s1 % PD][1]);
          if (s1 + PD < 28) { pre[s1 % PD][0] = ld16(rsG, wo + (s1 + PD) * 57344); pre[s1 % PD][1] = ld16(rsU, wo + (s1 + PD) * 57344); }
        }
        SB();
        ag[1] = mfma(wf[c][0], asb(xf[c][1]), ag[1]); au[1] = mfma(wf[c][1], asb(xf[c][1]), au[1]);
        SB();
        if (s1 < 28) {
          wf[n][0] = trfrag(slot + tro); wf[n][1] = trfrag(slot + 1024 + tro);
          xf[n][0] = lds16(sm + L_X + xa[s1 & 3] + (s1 >> 2) * 128);
        }
        SB();
        ag[2] = mfma(wf[c][0], asb(xf[c][2]), ag[2]); au[2] = mfma(wf[c][1], asb(xf[c][2]), au[2]);
        SB();
        if (s1 < 28) {
#pragma unroll
          for (int t = 1; t < 4; t++) xf[n][t] = lds16(sm + L_X + t * 28672 + xa[s1 & 3] + (s1 >> 2) * 128);
        }
        SB();
        ag[3] = mfma(wf[c][0], asb(xf[c][3]), ag[3]); au[3] = mfma(wf[c][1], asb(xf[c][3]), au[3]);
      }
      SB();
      lds_signal(sm, 1, lane);
      STAMP(2);
      __builtin_amdgcn_s_setprio(2);
      lds_wait(sm, 0, 4);
#pragma unroll
      for (int t = 0; t < 4; t++) {
        const float om = *(LDSP float*)(sm + L_OMG + (t * 32 + r) * 4);
#pragma unroll
        for (int e = 0; e < 16; e++) ag[t][e] *= au[t][e] * om;
        const unsigned hb = WS_H + ((4 * tb + t) * 112 + (n0 >> 4)) * 1024 + lane * 16;
        st16c(rsW, hb, packf(ag[t], 0));
        st16c(rsW, hb + 1024, packf(ag[t], 1));
      }
      publish(ws, WS_FH + (tb * 56 + 4 * j + wb) * FSTR, tag, lane);
      STAMP(3);
    } else {
      const bool isV = j == 15;
      const rsrc_t rsK = mkrs(isV ? P.wv : P.wk, 448 * 56);
      u32x4 v[28];
      if (!KVFIRST) load_x();
#pragma unroll
      for (int s = 0; s < 28; s++) v[s] = ld16(rsK, lane < 56 ? s * 896 + lane * 16 : OOB);
      SB();
      if (KVFIRST) { load_x(); asm volatile("s_waitcnt vmcnt(0)" ::: "memory"); } else asm volatile("s_waitcnt vmcnt(28)" ::: "memory");
      __builtin_amdgcn_s_barrier();
      asm volatile("" ::: "memory");
      tag = tagraw + 1;
      STAMP(1);
      __builtin_amdgcn_s_setprio(3);
      f32x16 a = {};
      const unsigned tro56 = (8 * (lane >> 5) + ((lane & 15) >> 2)) * 56 + 32 * ((lane >> 4) & 1) + 8 * (lane & 3);
      const unsigned slb = opq(sbase + L_BS + wb * 2048), slw = opq(slb + lane * 16), slr = opq(slb + tro56);
      unsigned xq[4];
#pragma unroll
      for (int u = 0; u < 4; u++) xq[u] = opq(sbase + L_X + wb * 28672 + xa[u]);
      f32x16 a2 = {};
      auto kvgemm = [&](auto VT) {
        constexpr int PQ = 4;
        bf16x8 wf[PQ]; u32x4 xf[PQ];
        auto stage = [&](int s) {
          lds16w(lp(slw) + (s & 1) * 1024, v[s]);
          s16x4 t0 = __builtin_amdgcn_ds_read_tr16_b64_v4i16((LDSP s16x4*)(lp(slr) + (s & 1) * 1024));
          s16x4 t1 = __builtin_amdgcn_ds_read_tr16_b64_v4i16((LDSP s16x4*)(lp(slr) + (s & 1) * 1024 + 224));
          u32x2 ua = __builtin_bit_cast(u32x2, t0), ub = __builtin_bit_cast(u32x2, t1);
          wf[s % PQ] = asb(u32x4{ua[0], ua[1], ub[0], ub[1]});
          xf[s % PQ] = lds16(lp(xq[s & 3]) + (s >> 2) * 128);
        };
#pragma unroll
        for (int s = 0; s < PQ; s++) { SB(); stage(s); }
#pragma unroll
        for (int s = 0; s < 28; s++) {
          SB();
          f32x16& ac = (s & 1) ? a2 : a;
          if (VT.value) ac = mfma(asb(xf[s % PQ]), wf[s % PQ], ac); else ac = mfma(wf[s % PQ], asb(xf[s % PQ]), ac);
          SB();
          if (s + PQ < 28) stage(s + PQ);
        }
        SB();
      };
      if (isV) kvgemm(BoolC<true>{}); else kvgemm(BoolC<false>{});
#pragma unroll
      for (int e = 0; e < 16; e++) a[e] += a2[e];
      lds_signal(sm, 1, lane);
      STAMP(2);
      const int T = 4 * tb + wb;
      if (!isV) {
        float n2 = 0.f;
#pragma unroll
        for (int e = 0; e < 16; e++) {
          if (e >= 12 && h) a[e] = 0.f;
          n2 += a[e] * a[e];
          if ((e == 12 || e == 13) && h) a[e] = 1.f;
        }
        n2 += xor32(n2);
        n2 = wave_max(n2);
        if (h) a[14] = fminf(n2 * 1.0078125f, 3.0e38f);
      } else {
#pragma unroll
        for (int e = 0; e < 16; e++) a[e] = r < 28 ? a[e] : (r == 28 ? 1.f : 0.f);
      }
      const unsigned kb = WS_KV + T * 4096 + (isV ? 2048 : 0) + lane * 16;
      st16c(rsW, kb, packf(a, 0));
      st16c(rsW, kb + 1024, packf(a, 1));
      STAMP(8);
      publish(ws, WS_FKV + (T * 2 + (isV ? 1 : 0)) * FSTR, tag, lane);
      STAMP(3);
    }
    __builtin_amdgcn_s_setprio(PPRIO);
    if (ii < 28) {
      constexpr int PD = P3PD;
      const int t64 = 4 * xcd + ii / 7, cb = ii % 7;
      const int T0 = 2 * t64;
      unsigned tc[3];
      {
        const int ks = lane < 28 ? 28 * wb + lane : 112 + 8 * wb + (lane - 28);
        poll(ws, WS_FW + (ks * 7 + cb) * FSTR, lane < 36, tag);
        const int li = (ii / 7) * 112 + lane, li2 = li + 64;
        tc[0] = __builtin_amdgcn_raw_buffer_load_b32(rsW, WS_W3 + ((28 * wb + (li >> 4)) * 14 + 2 * cb) * 1024 + (li & 15) * 128, 0, AUX_SC1);
        tc[1] = __builtin_amdgcn_raw_buffer_load_b32(rsW, lane < 48 ? WS_W3 + ((28 * wb + (li2 >> 4)) * 14 + 2 * cb) * 1024 + (li2 & 15) * 128 : OOB, 0, AUX_SC1);
        poll(ws, WS_FH + ((t64 >> 1) * 56 + 14 * wb + lane) * FSTR, lane < 14, tag);
        const int lh = cb * 64 + lane;
        tc[2] = __builtin_amdgcn_raw_buffer_load_b32(rsW, WS_H + ((2 * t64 + (lh >= 224)) * 112 + 28 * wb) * 1024 + (lh >= 224 ? lh - 224 : lh) * 128, 0, AUX_SC1);
      }
      STAMP(4);
      f32x16 acc[2][2] = {};
      const unsigned hb0 = WS_H + (T0 * 112 + 28 * wb) * 1024 + lane * 16, hb1 = hb0 + 112 * 1024;
      const unsigned wb0 = WS_W3 + (28 * wb * 14 + 2 * cb) * 1024 + lane * 16;
      u32x4 fr[PD][4];
#pragma unroll
      for (int s = 0; s < PD; s++) {
        fr[s][0] = ld16c(rsW, hb0 + s * 1024); fr[s][1] = ld16c(rsW, hb1 + s * 1024);
        fr[s][2] = ld16c(rsW, wb0 + s * 14336); fr[s][3] = ld16c(rsW, wb0 + s * 14336 + 1024);
      }
#pragma unroll
      for (int s = 0; s < 28; s++) {
        const int c = s % PD;
        SB();
        if (s == 1) asm volatile("" ::"v"(tc[0]), "v"(tc[1]), "v"(tc[2]));
        acc[0][0] = mfma(asb(fr[c][2]), asb(fr[c][0]), acc[0][0]);
        acc[0][1] = mfma(asb(fr[c][2]), asb(fr[c][1]), acc[0][1]);
        acc[1][0] = mfma(asb(fr[c][3]), asb(fr[c][0]), acc[1][0]);
        acc[1][1] = mfma(asb(fr[c][3]), asb(fr[c][1]), acc[1][1]);
        SB();
        if (s + PD < 28) {
          fr[c][0] = ld16c(rsW, hb0 + (s + PD) * 1024); fr[c][1] = ld16c(rsW, hb1 + (s + PD) * 1024);
          fr[c][2] = ld16c(rsW, wb0 + (s + PD) * 14336); fr[c][3] = ld16c(rsW, wb0 + (s + PD) * 14336 + 1024);
        }
      }
      SB();
      STAMP(5);
      const unsigned wo0 = WS_W3 + ((112 + 8 * wb) * 14 + 2 * cb) * 1024 + lane * 16;
      u32x4 wo_[8][2];
#pragma unroll
      for (int s = 0; s < 8; s++) { wo_[s][0] = ld16c(rsW, wo0 + s * 14336); wo_[s][1] = ld16c(rsW, wo0 + s * 14336 + 1024); }
      poll(ws, WS_FO + ((T0 + (lane >> 2)) * 16 + 4 * wb + (lane & 3)) * FSTR, lane < 8, tag);
      STAMP(6);
      const unsigned ob0 = WS_O + (T0 * 32 + 8 * wb) * 1024 + lane * 16, ob1 = ob0 + 32 * 1024;
      u32x4 of[8][2];
#pragma unroll
      for (int s = 0; s < 8; s++) { of[s][0] = ld16c(rsW, ob0 + s * 1024); of[s][1] = ld16c(rsW, ob1 + s * 1024); }
#pragma unroll
      for (int s = 0; s < 8; s++) {
        acc[0][0] = mfma(asb(wo_[s][0]), asb(of[s][0]), acc[0][0]);
        acc[0][1] = mfma(asb(wo_[s][0]), asb(of[s][1]), acc[0][1]);
        acc[1][0] = mfma(asb(wo_[s][1]), asb(of[s][0]), acc[1][0]);
        acc[1][1] = mfma(asb(wo_[s][1]), asb(of[s][1]), acc[1][1]);
      }
      lds_wait(sm, 0, 4);
      lds_wait(sm, 1, 4);
#pragma unroll
      for (int t = 0; t < 4; t++) {
        if (t != wb) {
#pragma unroll
          for (int qd = 0; qd < 4; qd++) {
            const f32x16& a = acc[t >> 1][t & 1];
            f32x4 v = {a[4 * qd], a[4 * qd + 1], a[4 * qd + 2], a[4 * qd + 3]};
            *(LDSP f32x4*)(sm + L_REDB + (wb * 4 + t) * 4096 + qd * 1024 + lane * 16) = v;
          }
        }
      }
      lds_signal(sm, 3, lane);
      f32x16 z = wb == 0 ? acc[0][0] : wb == 1 ? acc[0][1] : wb == 2 ? acc[1][0] : acc[1][1];
      lds_wait(sm, 3, 4);
#pragma unroll
      for (int w = 0; w < 4; w++) {
        if (w != wb) {
#pragma unroll
          for (int qd = 0; qd < 4; qd++) {
            f32x4 v = *(LDSP f32x4*)(sm + L_REDB + (w * 4 + wb) * 4096 + qd * 1024 + lane * 16);
            z[4 * qd] += v[0]; z[4 * qd + 1] += v[1]; z[4 * qd + 2] += v[2]; z[4 * qd + 3] += v[3];
          }
        }
      }
      const rsrc_t rsO = mkrs(P.out, 2048 * 896);
      const unsigned ob = ((T0 + (wb & 1)) * 32 + r) * 896 + (64 * cb + 32 * (wb >> 1) + 4 * h) * 2;
#pragma unroll
      for (int qd = 0; qd < 4; qd++) {
        u32x2 v = {pk2(z[4 * qd], z[4 * qd + 1]), pk2(z[4 * qd + 2], z[4 * qd + 3])};
        __builtin_amdgcn_raw_buffer_store_b64(v, rsO, ob + qd * 16, 0, 0);
      }
      STAMP(7);
    } else if (bid == 255 && wb == 3) {
      lds_wait(sm, 2, 4);
      for (;;) {
        u64 v[16];
#pragma unroll
        for (int i = 0; i < 16; i++) v[i] = __hip_atomic_load((const u64*)(ws + WS_FO + (i * 64 + lane) * FSTR), __ATOMIC_RELAXED, __HIP_MEMORY_SCOPE_AGENT);
        bool ok = true;
#pragma unroll
        for (int i = 0; i < 16; i++) ok = ok && v[i] == tag;
        if (__all(ok)) break;
        __builtin_amdgcn_s_sleep(8);
      }
      if (lane == 0) __hip_atomic_store((u64*)(ws + WS_EPOCH), tag, __ATOMIC_RELAXED, __HIP_MEMORY_SCOPE_AGENT);
      STAMP(10);
    }
  }
  STAMPS_OUT();
}

extern "C" void kernel_launch(void* const* d_in, const int* in_sizes, int n_in, void* d_out, int out_size, void* d_ws, size_t ws_size, hipStream_t stream) {
  Params P;
  P.x = (const char*)d_in[0]; P.wq = (const char*)d_in[1]; P.wk = (const char*)d_in[2]; P.wv = (const char*)d_in[3]; P.wo = (const char*)d_in[4];
  P.wg = (const char*)d_in[6]; P.wu = (const char*)d_in[7]; P.wd = (const char*)d_in[8]; P.gw = (const char*)d_in[9];
  P.out = (char*)d_out; P.ws = (char*)d_ws;
  fused_kernel<<<256, 512, 0, stream>>>(P);
}
```

```cpp
#include <hip/hip_runtime.h>
#include <hip/hip_bf16.h>
#include <stdint.h>

typedef __bf16 bf16x8 __attribute__((ext_vector_type(8)));
typedef __bf16 bf16x2 __attribute__((ext_vector_type(2)));
typedef short s16x4 __attribute__((ext_vector_type(4)));
typedef float f32x16 __attribute__((ext_vector_type(16)));
typedef float f32x4 __attribute__((ext_vector_type(4)));
typedef unsigned u32x4 __attribute__((ext_vector_type(4)));
typedef unsigned u32x2 __attribute__((ext_vector_type(2)));
typedef unsigned long long u64;
typedef __amdgpu_buffer_rsrc_t rsrc_t;
#define LDSP __attribute__((address_space(3)))
#define DEV __device__ __forceinline__
#define SB() __builtin_amdgcn_sched_barrier(0)

constexpr unsigned WS_EPOCH = 0;
#define FSTR 64
#define HPRIO 2
#define PPRIO 0
#define APRIO 1
#define KVFIRST 0
#define P3PD 8
#define SLP 4
#define CPF 1
#define TAB 0
#define DPPR 1
#define QPD 6
#define Q2 1
constexpr unsigned WS_FLAGS = 11829248;
constexpr unsigned WS_FKV = WS_FLAGS;
constexpr unsigned WS_FH = WS_FKV + 128 * FSTR;
constexpr unsigned WS_FO = WS_FH + 896 * FSTR;
constexpr unsigned WS_FW = WS_FO + 1024 * FSTR;
constexpr unsigned WS_KV = 65536;
constexpr unsigned WS_O = 327680;
constexpr unsigned WS_W3 = 2424832;
constexpr unsigned WS_H = 4489216;
constexpr unsigned WS_END = WS_FLAGS;
constexpr unsigned PROF_OFF = 12582912;
constexpr unsigned L_X = 0;
constexpr unsigned L_WQ = 114688;
constexpr unsigned L_QEX = 143360;
constexpr unsigned L_BS = 151552;
constexpr unsigned L_CNT = 159744;
constexpr unsigned L_FB = 160000 - 160000 % 256 + 256;
constexpr unsigned L_OMG = L_FB + 2048;
constexpr unsigned L_REDA = 0;
constexpr unsigned L_REDB = 65536;
constexpr unsigned LDS_BYTES = 163840;
constexpr int AUX_SC1 = 16;
#define NTB 8
constexpr unsigned OOB = 0x80000000u;
#define CPFL 30000
constexpr unsigned CPFLEN = CPFL;

#define STAMP(e)
#define STAMPC(e)
#define STAMPS_OUT()

DEV unsigned opq(unsigned v) { asm volatile("" : "+v"(v)); return v; }
DEV LDSP char* lp(unsigned a) { return (LDSP char*)(size_t)a; }
DEV rsrc_t mkrs(const void* p, unsigned bytes) { return __builtin_amdgcn_make_buffer_rsrc((void*)p, (short)0, (int)bytes, 0x00020000); }
DEV u32x4 ld16(rsrc_t r, unsigned off) { return __builtin_amdgcn_raw_buffer_load_b128(r, off, 0, 0); }
DEV u32x4 ld16c(rsrc_t r, unsigned off) { return __builtin_amdgcn_raw_buffer_load_b128(r, off, 0, AUX_SC1); }
DEV void st16c(rsrc_t r, unsigned off, u32x4 v) { __builtin_amdgcn_raw_buffer_store_b128(v, r, off, 0, AUX_SC1); }
DEV unsigned pk2(float a, float b) { bf16x2 v = {(__bf16)a, (__bf16)b}; return __builtin_bit_cast(unsigned, v); }
DEV bf16x8 asb(u32x4 v) { return __builtin_bit_cast(bf16x8, v); }
DEV f32x16 mfma(bf16x8 a, bf16x8 b, f32x16 c) { return __builtin_amdgcn_mfma_f32_32x32x16_bf16(a, b, c, 0, 0, 0); }
DEV u32x4 packf(const f32x16& a, int s2) {
  u32x4 r;
  r[0] = pk2(a[8 * s2 + 0], a[8 * s2 + 1]); r[1] = pk2(a[8 * s2 + 2], a[8 * s2 + 3]);
  r[2] = pk2(a[8 * s2 + 4], a[8 * s2 + 5]); r[3] = pk2(a[8 * s2 + 6], a[8 * s2 + 7]);
  return r;
}
DEV u32x4 lds16(LDSP char* p) { return *(LDSP u32x4*)p; }
DEV void lds16w(LDSP char* p, u32x4 v) { *(LDSP u32x4*)p = v; }
DEV bf16x8 trfrag(LDSP char* p) {
  s16x4 a = __builtin_amdgcn_ds_read_tr16_b64_v4i16((LDSP s16x4*)p);
  s16x4 b = __builtin_amdgcn_ds_read_tr16_b64_v4i16((LDSP s16x4*)(p + 256));
  u32x2 ua = __builtin_bit_cast(u32x2, a), ub = __builtin_bit_cast(u32x2, b);
  u32x4 r = {ua[0], ua[1], ub[0], ub[1]};
  return asb(r);
}
DEV void swap32(float& a, float& b) { asm volatile("s_nop 1\n\tv_permlane32_swap_b32 %0, %1" : "+v"(a), "+v"(b)); }
DEV void swap16(float& a, float& b) { asm volatile("s_nop 1\n\tv_permlane16_swap_b32 %0, %1" : "+v"(a), "+v"(b)); }
DEV float sum_halves(float v) { float a = v, b = v; swap32(a, b); return a + b; }
DEV float max_halves(float v) { float a = v, b = v; swap32(a, b); return fmaxf(a, b); }
template <int C> DEV float dppf(float v) { return __builtin_bit_cast(float, __builtin_amdgcn_update_dpp(0, __builtin_bit_cast(int, v), C, 0xf, 0xf, true)); }
DEV float wave_max32(float v) {
  v = fmaxf(v, dppf<0xB1>(v)); v = fmaxf(v, dppf<0x4E>(v)); v = fmaxf(v, dppf<0x141>(v)); v = fmaxf(v, dppf<0x140>(v));
  float a = v, b = v;
  swap16(a, b);
  return fmaxf(a, b);
}
DEV float wave_max(float v) { return max_halves(wave_max32(v)); }
DEV void poll(const char* ws, unsigned off, bool active, u64 tag) {
  for (;;) {
    u64 v = tag;
    if (active) v = __hip_atomic_load((const u64*)(ws + off), __ATOMIC_RELAXED, __HIP_MEMORY_SCOPE_AGENT);
    if (__all(v == tag)) break;
    __builtin_amdgcn_s_sleep(SLP);
  }
  asm volatile("" ::: "memory");
}
DEV void poll2(const char* ws, unsigned off1, bool act1, unsigned off2, bool act2, u64 tag) {
  for (;;) {
    u64 v1 = tag, v2 = tag;
    if (act1) v1 = __hip_atomic_load((const u64*)(ws + off1), __ATOMIC_RELAXED, __HIP_MEMORY_SCOPE_AGENT);
    if (act2) v2 = __hip_atomic_load((const u64*)(ws + off2), __ATOMIC_RELAXED, __HIP_MEMORY_SCOPE_AGENT);
    if (__all(v1 == tag && v2 == tag)) break;
    __builtin_amdgcn_s_sleep(1);
  }
  asm volatile("" ::: "memory");
}
DEV void publish(char* ws, unsigned off, u64 tag, int lane) {
  asm volatile("s_waitcnt vmcnt(0)" ::: "memory");
  if (lane == 0) __hip_atomic_store((u64*)(ws + off), tag, __ATOMIC_RELAXED, __HIP_MEMORY_SCOPE_AGENT);
}
DEV void lds_signal(LDSP char* sm, int idx, int lane) {
  asm volatile("s_waitcnt lgkmcnt(0)" ::: "memory");
  if (lane == 0) __hip_atomic_fetch_add((LDSP unsigned*)(sm + L_CNT + idx * 4), 1u, __ATOMIC_RELAXED, __HIP_MEMORY_SCOPE_WORKGROUP);
}
DEV void lds_wait(LDSP char* sm, int idx, unsigned n) {
  while (__builtin_amdgcn_readfirstlane(__hip_atomic_load((LDSP unsigned*)(sm + L_CNT + idx * 4), __ATOMIC_RELAXED, __HIP_MEMORY_SCOPE_WORKGROUP)) < n)
    __builtin_amdgcn_s_sleep(1);
  asm volatile("" ::: "memory");
}

template <bool B> struct BoolC { static constexpr bool value = B; };
struct Params {
  const char *x, *wq, *wk, *wv, *wo, *wg, *wu, *wd, *gw;
  char *out, *ws;
};

__global__ __launch_bounds__(512, 2) void fused_kernel(Params P) {
  __shared__ __attribute__((aligned(1024))) char smem_[LDS_BYTES];
  LDSP char* sm = (LDSP char*)smem_;
  char* ws = P.ws;
  const unsigned sbase = (unsigned)(size_t)sm;
  const int lane = threadIdx.x & 63;
  const int wave = __builtin_amdgcn_readfirstlane(threadIdx.x >> 6);
  const int bid = blockIdx.x;
  const int xcd = bid & 7, ii = bid >> 3;
  const int tb = (xcd / (NTB / 2)) * NTB + (ii % NTB), j = (xcd % (NTB / 2)) * (32 / NTB) + ii / NTB;
  const int r = lane & 31, h = lane >> 5;
  const rsrc_t rsW = mkrs(ws, WS_END);
  STAMP(0); STAMPC(14);
  u64 tag = 0, tagraw = 0;
  auto load_x = [&]() {
    const rsrc_t rsX = mkrs(P.x, 2048 * 896);
#pragma unroll
    for (int i = 0; i < 14; i++) {
      const int S = (wave * 14 + i) * 64 + lane;
      const int rr = S / 56, s = S - rr * 56;
      const int c = (s & ~7) | ((s & 7) ^ ((rr >> 1) & 7));
      __builtin_amdgcn_raw_ptr_buffer_load_lds(rsX, (LDSP void*)(sm + L_X + (wave * 14 + i) * 1024), 16, (tb * 128 + rr) * 896 + c * 16, 0, 0, 0);
    }
    tagraw = __hip_atomic_load((const u64*)(ws + WS_EPOCH), __ATOMIC_RELAXED, __HIP_MEMORY_SCOPE_AGENT);
    SB();
  };
  unsigned cpf = 0;
  if (CPF && ii < 8 && wave < 4) {
    u64 pc;
    asm volatile("s_getpc_b64 %0" : "=s"(pc));
    cpf = __builtin_amdgcn_raw_buffer_load_b32(mkrs((const void*)(pc & ~(u64)255), CPFLEN), (wave * 64 + lane) * 128, 0, 0);
  }
  if (threadIdx.x < 8) *(LDSP unsigned*)(sm + L_CNT + threadIdx.x * 4) = 0;
  unsigned xa[4];
#pragma unroll
  for (int u = 0; u < 4; u++) xa[u] = r * 896 + (((2 * u + h) ^ ((r >> 1) & 7)) << 4);
  const unsigned tro = (8 * (lane >> 5) + ((lane & 15) >> 2)) * 64 + 32 * ((lane >> 4) & 1) + 8 * (lane & 3);

  if (wave < 4) {
    const int wa = wave;
    const int pw = bid * 4 + wa;
    u32x4 w3[2];
    {
      const rsrc_t rsQ = mkrs(P.wq, 448 * 896), rsG = mkrs(P.gw, 896);
      load_x();
      const int u = lane & 7;
      u32x2 v[14];
      const unsigned qo = u < 7 ? (wa * 8 + (lane >> 3)) * 896 + 56 * j + 8 * u : OOB;
#pragma unroll
      for (int it = 0; it < 14; it++) v[it] = __builtin_amdgcn_raw_buffer_load_b64(rsQ, qo + it * 32 * 896, 0, 0);
      const int tid = wa * 64 + lane;
      const unsigned g0 = __builtin_amdgcn_raw_buffer_load_b16(rsG, tid * 2, 0, 0);
      const unsigned g1 = __builtin_amdgcn_raw_buffer_load_b16(rsG, (tid + 256) * 2, 0, 0);
      SB();
      {
        const int ir = lane >> 2, kk = (ir & 3) | ((ir & 4) << 1) | ((ir & 8) >> 1);
        const int ks = pw / 7, cb = pw - 7 * ks, ko = ks - 112;
        const bool isd = ks < 112;
        const int row0 = isd ? 16 * ks : 28 * (ko >> 1) + 16 * (ko & 1);
        const unsigned vo = (pw >= 1008 || (!isd && (ko & 1) && kk >= 12)) ? OOB : kk * 896 + (lane & 3) * 16;
        const rsrc_t rs3 = mkrs(isd ? P.wd : P.wo, isd ? 1792 * 896 : 448 * 896);
        w3[0] = __builtin_amdgcn_raw_buffer_load_b128(rs3, vo, pw >= 1008 ? 0 : row0 * 896 + cb * 128, 0);
        w3[1] = __builtin_amdgcn_raw_buffer_load_b128(rs3, vo, pw >= 1008 ? 0 : row0 * 896 + cb * 128 + 64, 0);
      }
      SB();
      if (u < 7) {
#pragma unroll
        for (int it = 0; it < 14; it++) *(LDSP u32x2*)(sm + L_WQ + (it * 256 + wa * 64 + lane) * 8) = v[it];
      }
      *(LDSP u32x2*)(sm + L_WQ + tid * 64 + 56) = u32x2{g0, 0u};
      if (tid < 192) *(LDSP u32x2*)(sm + L_WQ + (tid + 256) * 64 + 56) = u32x2{g1, 0u};
    }
    asm volatile("s_waitcnt vmcnt(2) lgkmcnt(0)" ::: "memory");
    __builtin_amdgcn_s_barrier();
    asm volatile("" ::: "memory");
    tag = tagraw + 1;
    asm volatile("" ::"v"(cpf));
    STAMP(1);
    if (pw < 1008) {
      LDSP char* slot = sm + L_QEX + wa * 2048;
      lds16w(slot + lane * 16, w3[0]); lds16w(slot + 1024 + lane * 16, w3[1]);
      const unsigned dst = WS_W3 + ((pw / 7) * 14 + 2 * (pw % 7)) * 1024 + lane * 16;
      st16c(rsW, dst, __builtin_bit_cast(u32x4, trfrag(slot + tro)));
      st16c(rsW, dst + 1024, __builtin_bit_cast(u32x4, trfrag(slot + 1024 + tro)));
    }
    float gval;
    {
      __builtin_amdgcn_s_setprio(2);
      f32x16 q = {}, q2 = {};
      const unsigned wqb = opq(sbase + L_WQ + tro);
      unsigned xq[4];
#pragma unroll
      for (int u = 0; u < 4; u++) xq[u] = opq(sbase + L_X + wa * 28672 + xa[u]);
      constexpr int PQ = QPD;
      bf16x8 wf[PQ]; u32x4 xf[PQ];
#pragma unroll
      for (int s = 0; s < PQ; s++) { wf[s] = trfrag(lp(wqb) + s * 1024); xf[s] = lds16(lp(xq[s & 3]) + (s >> 2) * 128); }
#pragma unroll
      for (int s = 0; s < 28; s++) {
        SB();
        if (Q2 && (s & 1)) q2 = mfma(wf[s % PQ], asb(xf[s % PQ]), q2); else q = mfma(wf[s % PQ], asb(xf[s % PQ]), q);
        SB();
        if (s + PQ < 28) { wf[s % PQ] = trfrag(lp(wqb) + (s + PQ) * 1024); xf[s % PQ] = lds16(lp(xq[(s + PQ) & 3]) + ((s + PQ) >> 2) * 128); }
      }
      SB();
      if (Q2) {
#pragma unroll
        for (int e = 0; e < 16; e++) q[e] += q2[e];
      }
      const float logit = h ? q[12] : 0.f;
      gval = 1.f / (1.f + __expf(-sum_halves(logit)));
      const float qs = 0.18898223650461362f * 1.4426950408889634f;
      float n2 = 0.f;
#pragma unroll
      for (int e = 0; e < 16; e++) {
        q[e] *= qs;
        if (e >= 12 && h) q[e] = 0.f;
        n2 += q[e] * q[e];
      }
      lds16w(sm + L_QEX + wa * 2048 + lane * 16, packf(q, 0));
      lds16w(sm + L_QEX + wa * 2048 + 1024 + lane * 16, packf(q, 1));
      n2 = sum_halves(n2);
      n2 = wave_max32(n2);
      if (lane == 0) *(LDSP float*)(sm + L_CNT + 32 + wa * 4) = n2;
      if (!h) *(LDSP float*)(sm + L_OMG + (wa * 32 + r) * 4) = 1.f - gval;
      lds_signal(sm, 0, lane);
    }
    STAMP(2);
    lds_wait(sm, 0, 4);
    __builtin_amdgcn_s_setprio(APRIO);
    u32x4 qf[4][2];
#pragma unroll
    for (int t = 0; t < 4; t++) { qf[t][0] = lds16(sm + L_QEX + t * 2048 + lane * 16); qf[t][1] = lds16(sm + L_QEX + t * 2048 + 1024 + lane * 16); }
    f32x4 qm = *(LDSP f32x4*)(sm + L_CNT + 32);
    const float qmax2 = fmaxf(fmaxf(qm[0], qm[1]), fmaxf(qm[2], qm[3]));
    STAMP(6);
    poll(ws, WS_FKV + (32 * wa + (lane & 31)) * FSTR, lane < 32, tag);
    if (pw < 1008) publish(ws, WS_FW + pw * FSTR, tag, lane);
    STAMP(3); STAMPC(12);
    const unsigned kvb = WS_KV + 16 * wa * 4096 + lane * 16;
    u32x4 K[4][2], V[4][2];
    const float kmx = __builtin_bit_cast(float, __builtin_amdgcn_raw_buffer_load_b32(rsW, WS_KV + (16 * wa + (lane & 15)) * 4096 + 1024 + 32 * 16 + 12, 0, AUX_SC1) << 16);
#pragma unroll
    for (int a = 0; a < 4; a++) {
      K[a][0] = ld16c(rsW, kvb + a * 4096); K[a][1] = ld16c(rsW, kvb + a * 4096 + 1024);
      V[a][0] = ld16c(rsW, kvb + a * 4096 + 2048); V[a][1] = ld16c(rsW, kvb + a * 4096 + 3072);
    }
    const unsigned tch = __builtin_amdgcn_raw_buffer_load_b32(rsW, WS_KV + 16 * wa * 4096 + (ii & 7) * 8192 + lane * 128, 0, AUX_SC1);
    const float kmax2 = wave_max(kmx);
    float cref[4] = {0.f, 0.f, 0.f, 0.f};
    if (!(qmax2 * kmax2 <= 3600.f)) {
      float mx[4] = {-3.0e38f, -3.0e38f, -3.0e38f, -3.0e38f};
#pragma unroll 1
      for (int kt = 0; kt < 16; kt++) {
        const u32x4 k0 = ld16c(rsW, kvb + kt * 4096), k1 = ld16c(rsW, kvb + kt * 4096 + 1024);
#pragma unroll
        for (int t = 0; t < 4; t++) {
          f32x16 s = {};
          s = mfma(asb(k0), asb(qf[t][0]), s);
          s = mfma(asb(k1), asb(qf[t][1]), s);
#pragma unroll
          for (int e = 0; e < 16; e++) mx[t] = fmaxf(mx[t], s[e]);
        }
      }
#pragma unroll
      for (int t = 0; t < 4; t++) {
        const float c = max_halves(mx[t]);
        const float chi = (float)(__bf16)c, clo = (float)(__bf16)(c - chi);
        cref[t] = chi + clo;
        if (h) qf[t][1][2] = pk2(-chi, -clo);
      }
    }
    if (!h) {
#pragma unroll
      for (int t = 0; t < 4; t++) *(LDSP float*)(sm + L_FB + (wa * 4 + t) * 128 + r * 4) = cref[t];
    }
    f32x16 o[4] = {};
    {
      const f32x16 zero = {};
      f32x16 S0, S1;
      u32x4 P0, PA1, PB1 = {0u, 0u, 0u, 0u};
      S0 = mfma(asb(K[0][0]), asb(qf[0][0]), zero);
      S0 = mfma(asb(K[0][1]), asb(qf[0][1]), S0);
      asm volatile("" ::"v"(tch));
#define UNIT(Sc, Sn, Kn, tn, Vp, Pp, tp, Vc, Pc, tc)                                                     \
      SB(); Sn = mfma(asb(Kn[0]), asb(qf[tn][0]), zero); SB();                                            \
      EXP4(Sc, 0); P0[0] = pk2(Sc[0], Sc[1]); P0[1] = pk2(Sc[2], Sc[3]);                                  \
      SB(); Sn = mfma(asb(Kn[1]), asb(qf[tn][1]), Sn); SB();                                              \
      EXP4(Sc, 4); P0[2] = pk2(Sc[4], Sc[5]); P0[3] = pk2(Sc[6], Sc[7]);                                  \
      SB(); o[tp] = mfma(asb(Vp[1]), asb(Pp), o[tp]); SB();                                               \
      EXP4(Sc, 8); Pc[0] = pk2(Sc[8], Sc[9]); Pc[1] = pk2(Sc[10], Sc[11]);                                \
      SB(); o[tc] = mfma(asb(Vc[0]), asb(P0), o[tc]); SB();                                               \
      EXP4(Sc, 12); Pc[2] = pk2(Sc[12], Sc[13]); Pc[3] = pk2(Sc[14], Sc[15]);
#define EXP4(S, b) S[b] = __builtin_amdgcn_exp2f(S[b]); S[b + 1] = __builtin_amdgcn_exp2f(S[b + 1]); S[b + 2] = __builtin_amdgcn_exp2f(S[b + 2]); S[b + 3] = __builtin_amdgcn_exp2f(S[b + 3]);
#define TILE(a, am, ap)                                                                                   \
        UNIT(S0, S1, K[a], 1, V[am], PB1, 3, V[a], PA1, 0)                                                \
        SB(); { const unsigned vo = (4 * it + a + 3 < 16) ? kvb + (4 * it + a + 3) * 4096 + 2048 : OOB;   \
                V[am][0] = ld16c(rsW, vo); V[am][1] = ld16c(rsW, vo + 1024); }                            \
        UNIT(S1, S0, K[a], 2, V[a], PA1, 0, V[a], PB1, 1)                                                 \
        UNIT(S0, S1, K[a], 3, V[a], PB1, 1, V[a], PA1, 2)                                                 \
        SB(); { const unsigned ko = (4 * it + a + 4 < 16) ? kvb + (4 * it + a + 4) * 4096 : OOB;          \
                K[a][0] = ld16c(rsW, ko); K[a][1] = ld16c(rsW, ko + 1024); }                              \
        UNIT(S1, S0, K[ap], 0, V[a], PA1, 2, V[a], PB1, 3)
#pragma unroll 1
      for (int it = 0; it < 4; it++) {
        TILE(0, 3, 1)
        TILE(1, 0, 2)
        TILE(2, 1, 3)
        TILE(3, 2, 0)
      }
      SB();
      o[3] = mfma(asb(V[3][1]), asb(PB1), o[3]);
    }
    STAMP(4); STAMPC(13);
    lds_wait(sm, 1, 4);
#pragma unroll
    for (int t = 0; t < 4; t++) {
      if (t != wa) {
#pragma unroll
        for (int qd = 0; qd < 4; qd++) {
          f32x4 v = {o[t][4 * qd], o[t][4 * qd + 1], o[t][4 * qd + 2], o[t][4 * qd + 3]};
          *(LDSP f32x4*)(sm + L_REDA + (wa * 4 + t) * 4096 + qd * 1024 + lane * 16) = v;
        }
      }
    }
    lds_signal(sm, 2, lane);
    f32x16 of = wa == 0 ? o[0] : wa == 1 ? o[1] : wa == 2 ? o[2] : o[3];
    lds_wait(sm, 2, 4);
    {
      float cw[4];
#pragma unroll
      for (int w = 0; w < 4; w++) cw[w] = *(LDSP float*)(sm + L_FB + (w * 4 + wa) * 128 + r * 4);
      const float cm = fmaxf(fmaxf(cw[0], cw[1]), fmaxf(cw[2], cw[3]));
      const float cown = wa == 0 ? cw[0] : wa == 1 ? cw[1] : wa == 2 ? cw[2] : cw[3];
      const float fo = __builtin_amdgcn_exp2f(cown - cm);
#pragma unroll
      for (int e = 0; e < 16; e++) of[e] *= fo;
#pragma unroll
      for (int w = 0; w < 4; w++) {
        if (w != wa) {
          const float f = __builtin_amdgcn_exp2f(cw[w] - cm);
#pragma unroll
          for (int qd = 0; qd < 4; qd++) {
            f32x4 v = *(LDSP f32x4*)(sm + L_REDA + (w * 4 + wa) * 4096 + qd * 1024 + lane * 16);
            of[4 * qd] += f * v[0]; of[4 * qd + 1] += f * v[1]; of[4 * qd + 2] += f * v[2]; of[4 * qd + 3] += f * v[3];
          }
        }
      }
    }
    {
      const float lh = h ? of[12] : 0.f;
      const float sc = gval / sum_halves(lh);
#pragma unroll
      for (int e = 0; e < 16; e++) { of[e] *= sc; if (e >= 12 && h) of[e] = 0.f; }
      const int T = 4 * tb + wa;
      st16c(rsW, WS_O + (T * 32 + 2 * j) * 1024 + lane * 16, packf(of, 0));
      st16c(rsW, WS_O + (T * 32 + 2 * j + 1) * 1024 + lane * 16, packf(of, 1));
      publish(ws, WS_FO + (T * 16 + j) * FSTR, tag, lane);
    }
    STAMP(5); STAMPC(15);
  } else {
    const int wb = wave - 4;
    LDSP char* slot = sm + L_BS + wb * 2048;
    if (j < 14) {
      constexpr int PD = 6;
      const rsrc_t rsG = mkrs(P.wg, 448 * 3584), rsU = mkrs(P.wu, 448 * 3584);
      const unsigned n0 = 128 * j + 32 * wb;
      const unsigned wo = (lane >> 2) * 3584 + (lane & 3) * 16 + n0 * 2;
      load_x();
      unsigned tw;
      if (!TAB) {
        constexpr int CNT = 448 / NTB;
        const int li = ((ii % NTB) * 4 + wb) * CNT + lane;
        tw = __builtin_amdgcn_raw_buffer_load_b32((li & 2) ? rsU : rsG, lane < CNT ? (li >> 2) * 3584 + 256 * j + (li & 1) * 128 : OOB, 0, 0);
      }
      SB();
      u32x4 pre[PD][2];
#pragma unroll
      for (int s = 0; s < PD; s++) { pre[s][0] = ld16(rsG, wo + s * 57344); pre[s][1] = ld16(rsU, wo + s * 57344); }
      asm volatile("s_waitcnt vmcnt(%0)" ::"n"(2 * PD + (TAB ? 0 : 1)) : "memory");
      __builtin_amdgcn_s_barrier();
      asm volatile("" ::: "memory");
      tag = tagraw + 1;
      STAMP(1);
      if (TAB) {
        constexpr int CNT = 448 / NTB;
        const int li = ((ii % NTB) * 4 + wb) * CNT + lane;
        tw = __builtin_amdgcn_raw_buffer_load_b32((li & 2) ? rsU : rsG, lane < CNT ? (li >> 2) * 3584 + 256 * j + (li & 1) * 128 : OOB, 0, 0);
      }
      SB();
      __builtin_amdgcn_s_setprio(HPRIO);
      f32x16 ag[4] = {}, au[4] = {};
      bf16x8 wf[2][2]; u32x4 xf[2][4];
      lds16w(slot + lane * 16, pre[0][0]); lds16w(slot + 1024 + lane * 16, pre[0][1]);
      asm volatile("" ::"v"(tw));
      pre[0][0] = ld16(rsG, wo + PD * 57344); pre[0][1] = ld16(rsU, wo + PD * 57344);
      wf[0][0] = trfrag(slot + tro); wf[0][1] = trfrag(slot + 1024 + tro);
#pragma unroll
      for (int t = 0; t < 4; t++) xf[0][t] = lds16(sm + L_X + t * 28672 + xa[0]);
#pragma unroll
      for (int s = 0; s < 28; s++) {
        const int c = s & 1, n = c ^ 1, s1 = s + 1;
        SB();
        ag[0] = mfma(wf[c][0], asb(xf[c][0]), ag[0]); au[0] = mfma(wf[c][1], asb(xf[c][0]), au[0]);
        SB();
        if (s1 < 28) {
          lds16w(slot + lane * 16, pre[s1 % PD][0]); lds16w(slot + 1024 + lane * 16, pre[s1 % PD][1]);
          if (s1 + PD < 28) { pre[s1 % PD][0] = ld16(rsG, wo + (s1 + PD) * 57344); pre[s1 % PD][1] = ld16(rsU, wo + (s1 + PD) * 57344); }
        }
        SB();
        ag[1] = mfma(wf[c][0], asb(xf[c][1]), ag[1]); au[1] = mfma(wf[c][1], asb(xf[c][1]), au[1]);
        SB();
        if (s1 < 28) {
          wf[n][0] = trfrag(slot + tro); wf[n][1] = trfrag(slot + 1024 + tro);
          xf[n][0] = lds16(sm + L_X + xa[s1 & 3] + (s1 >> 2) * 128);
        }
        SB();
        ag[2] = mfma(wf[c][0], asb(xf[c][2]), ag[2]); au[2] = mfma(wf[c][1], asb(xf[c][2]), au[2]);
        SB();
        if (s1 < 28) {
#pragma unroll
          for (int t = 1; t < 4; t++) xf[n][t] = lds16(sm + L_X + t * 28672 + xa[s1 & 3] + (s1 >> 2) * 128);
        }
        SB();
        ag[3] = mfma(wf[c][0], asb(xf[c][3]), ag[3]); au[3] = mfma(wf[c][1], asb(xf[c][3]), au[3]);
      }
      SB();
      lds_signal(sm, 1, lane);
      STAMP(2);
      __builtin_amdgcn_s_setprio(2);
      lds_wait(sm, 0, 4);
#pragma unroll
      for (int t = 0; t < 4; t++) {
        const float om = *(LDSP float*)(sm + L_OMG + (t * 32 + r) * 4);
#pragma unroll
        for (int e = 0; e < 16; e++) ag[t][e] *= au[t][e] * om;
        const unsigned hb = WS_H + ((4 * tb + t) * 112 + (n0 >> 4)) * 1024 + lane * 16;
        st16c(rsW, hb, packf(ag[t], 0));
        st16c(rsW, hb + 1024, packf(ag[t], 1));
      }
      publish(ws, WS_FH + (tb * 56 + 4 * j + wb) * FSTR, tag, lane);
      STAMP(3);
    } else {
      const bool isV = j == 15;
      const rsrc_t rsK = mkrs(isV ? P.wv : P.wk, 448 * 56);
      u32x4 v[28];
      if (!KVFIRST) load_x();
#pragma unroll
      for (int s = 0; s < 28; s++) v[s] = ld16(rsK, lane < 56 ? s * 896 + lane * 16 : OOB);
      SB();
      if (KVFIRST) { load_x(); asm volatile("s_waitcnt vmcnt(0)" ::: "memory"); } else asm volatile("s_waitcnt vmcnt(28)" ::: "memory");
      __builtin_amdgcn_s_barrier();
      asm volatile("" ::: "memory");
      tag = tagraw + 1;
      STAMP(1);
      __builtin_amdgcn_s_setprio(3);
      f32x16 a = {};
      const unsigned tro56 = (8 * (lane >> 5) + ((lane & 15) >> 2)) * 56 + 32 * ((lane >> 4) & 1) + 8 * (lane & 3);
      const unsigned slb = opq(sbase + L_BS + wb * 2048), slw = opq(slb + lane * 16), slr = opq(slb + tro56);
      unsigned xq[4];
#pragma unroll
      for (int u = 0; u < 4; u++) xq[u] = opq(sbase + L_X + wb * 28672 + xa[u]);
      f32x16 a2 = {};
      auto kvgemm = [&](auto VT) {
        constexpr int PQ = 4;
        bf16x8 wf[PQ]; u32x4 xf[PQ];
        auto stage = [&](int s) {
          lds16w(lp(slw) + (s & 1) * 1024, v[s]);
          s16x4 t0 = __builtin_amdgcn_ds_read_tr16_b64_v4i16((LDSP s16x4*)(lp(slr) + (s & 1) * 1024));
          s16x4 t1 = __builtin_amdgcn_ds_read_tr16_b64_v4i16((LDSP s16x4*)(lp(slr) + (s & 1) * 1024 + 224));
          u32x2 ua = __builtin_bit_cast(u32x2, t0), ub = __builtin_bit_cast(u32x2, t1);
          wf[s % PQ] = asb(u32x4{ua[0], ua[1], ub[0], ub[1]});
          xf[s % PQ] = lds16(lp(xq[s & 3]) + (s >> 2) * 128);
        };
#pragma unroll
        for (int s = 0; s < PQ; s++) { SB(); stage(s); }
#pragma unroll
        for (int s = 0; s < 28; s++) {
          SB();
          f32x16& ac = (s & 1) ? a2 : a;
          if (VT.value) ac = mfma(asb(xf[s % PQ]), wf[s % PQ], ac); else ac = mfma(wf[s % PQ], asb(xf[s % PQ]), ac);
          SB();
          if (s + PQ < 28) stage(s + PQ);
        }
        SB();
      };
      if (isV) kvgemm(BoolC<true>{}); else kvgemm(BoolC<false>{});
#pragma unroll
      for (int e = 0; e < 16; e++) a[e] += a2[e];
      lds_signal(sm, 1, lane);
      STAMP(2);
      const int T = 4 * tb + wb;
      if (!isV) {
        float n2 = 0.f;
#pragma unroll
        for (int e = 0; e < 16; e++) {
          if (e >= 12 && h) a[e] = 0.f;
          n2 += a[e] * a[e];
          if ((e == 12 || e == 13) && h) a[e] = 1.f;
        }
        n2 = sum_halves(n2);
        n2 = DPPR ? wave_max32(n2) : wave_max(n2);
        if (h) a[14] = fminf(n2 * 1.0078125f, 3.0e38f);
      } else {
#pragma unroll
        for (int e = 0; e < 16; e++) a[e] = r < 28 ? a[e] : (r == 28 ? 1.f : 0.f);
      }
      const unsigned kb = WS_KV + T * 4096 + (isV ? 2048 : 0) + lane * 16;
      st16c(rsW, kb, packf(a, 0));
      st16c(rsW, kb + 1024, packf(a, 1));
      STAMP(8);
      publish(ws, WS_FKV + (T * 2 + (isV ? 1 : 0)) * FSTR, tag, lane);
      STAMP(3);
    }
    __builtin_amdgcn_s_setprio(PPRIO);
    if (ii < 28) {
      constexpr int PD = P3PD;
      const int t64 = 4 * xcd + ii / 7, cb = ii % 7;
      const int T0 = 2 * t64;
      unsigned tc[3];
      {
        const int ks = lane < 28 ? 28 * wb + lane : 112 + 8 * wb + (lane - 28);
        poll(ws, WS_FW + (ks * 7 + cb) * FSTR, lane < 36, tag);
        const int li = (ii / 7) * 112 + lane, li2 = li + 64;
        tc[0] = __builtin_amdgcn_raw_buffer_load_b32(rsW, WS_W3 + ((28 * wb + (li >> 4)) * 14 + 2 * cb) * 1024 + (li & 15) * 128, 0, AUX_SC1);
        tc[1] = __builtin_amdgcn_raw_buffer_load_b32(rsW, lane < 48 ? WS_W3 + ((28 * wb + (li2 >> 4)) * 14 + 2 * cb) * 1024 + (li2 & 15) * 128 : OOB, 0, AUX_SC1);
        poll(ws, WS_FH + ((t64 >> 1) * 56 + 14 * wb + lane) * FSTR, lane < 14, tag);
        const int lh = cb * 64 + lane;
        tc[2] = __builtin_amdgcn_raw_buffer_load_b32(rsW, WS_H + ((2 * t64 + (lh >= 224)) * 112 + 28 * wb) * 1024 + (lh >= 224 ? lh - 224 : lh) * 128, 0, AUX_SC1);
      }
      STAMP(4);
      f32x16 acc[2][2] = {};
      const unsigned hb0 = WS_H + (T0 * 112 + 28 * wb) * 1024 + lane * 16, hb1 = hb0 + 112 * 1024;
      const unsigned wb0 = WS_W3 + (28 * wb * 14 + 2 * cb) * 1024 + lane * 16;
      u32x4 fr[PD][4];
#pragma unroll
      for (int s = 0; s < PD; s++) {
        fr[s][0] = ld16c(rsW, hb0 + s * 1024); fr[s][1] = ld16c(rsW, hb1 + s * 1024);
        fr[s][2] = ld16c(rsW, wb0 + s * 14336); fr[s][3] = ld16c(rsW, wb0 + s * 14336 + 1024);
      }
#pragma unroll
      for (int s = 0; s < 28; s++) {
        const int c = s % PD;
        SB();
        if (s == 1) asm volatile("" ::"v"(tc[0]), "v"(tc[1]), "v"(tc[2]));
        acc[0][0] = mfma(asb(fr[c][2]), asb(fr[c][0]), acc[0][0]);
        acc[0][1] = mfma(asb(fr[c][2]), asb(fr[c][1]), acc[0][1]);
        acc[1][0] = mfma(asb(fr[c][3]), asb(fr[c][0]), acc[1][0]);
        acc[1][1] = mfma(asb(fr[c][3]), asb(fr[c][1]), acc[1][1]);
        SB();
        if (s + PD < 28) {
          fr[c][0] = ld16c(rsW, hb0 + (s + PD) * 1024); fr[c][1] = ld16c(rsW, hb1 + (s + PD) * 1024);
          fr[c][2] = ld16c(rsW, wb0 + (s + PD) * 14336); fr[c][3] = ld16c(rsW, wb0 + (s + PD) * 14336 + 1024);
        }
      }
      SB();
      STAMP(5);
      const unsigned wo0 = WS_W3 + ((112 + 8 * wb) * 14 + 2 * cb) * 1024 + lane * 16;
      u32x4 wo_[8][2];
#pragma unroll
      for (int s = 0; s < 8; s++) { wo_[s][0] = ld16c(rsW, wo0 + s * 14336); wo_[s][1] = ld16c(rsW, wo0 + s * 14336 + 1024); }
      poll(ws, WS_FO + ((T0 + (lane >> 2)) * 16 + 4 * wb + (lane & 3)) * FSTR, lane < 8, tag);
      STAMP(6);
      const unsigned ob0 = WS_O + (T0 * 32 + 8 * wb) * 1024 + lane * 16, ob1 = ob0 + 32 * 1024;
      u32x4 of[8][2];
#pragma unroll
      for (int s = 0; s < 8; s++) { of[s][0] = ld16c(rsW, ob0 + s * 1024); of[s][1] = ld16c(rsW, ob1 + s * 1024); }
#pragma unroll
      for (int s = 0; s < 8; s++) {
        acc[0][0] = mfma(asb(wo_[s][0]), asb(of[s][0]), acc[0][0]);
        acc[0][1] = mfma(asb(wo_[s][0]), asb(of[s][1]), acc[0][1]);
        acc[1][0] = mfma(asb(wo_[s][1]), asb(of[s][0]), acc[1][0]);
        acc[1][1] = mfma(asb(wo_[s][1]), asb(of[s][1]), acc[1][1]);
      }
      lds_wait(sm, 0, 4);
      lds_wait(sm, 1, 4);
#pragma unroll
      for (int t = 0; t < 4; t++) {
        if (t != wb) {
#pragma unroll
          for (int qd = 0; qd < 4; qd++) {
            const f32x16& a = acc[t >> 1][t & 1];
            f32x4 v = {a[4 * qd], a[4 * qd + 1], a[4 * qd + 2], a[4 * qd + 3]};
            *(LDSP f32x4*)(sm + L_REDB + (wb * 4 + t) * 4096 + qd * 1024 + lane * 16) = v;
          }
        }
      }
      lds_signal(sm, 3, lane);
      f32x16 z = wb == 0 ? acc[0][0] : wb == 1 ? acc[0][1] : wb == 2 ? acc[1][0] : acc[1][1];
      lds_wait(sm, 3, 4);
#pragma unroll
      for (int w = 0; w < 4; w++) {
        if (w != wb) {
#pragma unroll
          for (int qd = 0; qd < 4; qd++) {
            f32x4 v = *(LDSP f32x4*)(sm + L_REDB + (w * 4 + wb) * 4096 + qd * 1024 + lane * 16);
            z[4 * qd] += v[0]; z[4 * qd + 1] += v[1]; z[4 * qd + 2] += v[2]; z[4 * qd + 3] += v[3];
          }
        }
      }
      const rsrc_t rsO = mkrs(P.out, 2048 * 896);
      const unsigned ob = ((T0 + (wb & 1)) * 32 + r) * 896 + (64 * cb + 32 * (wb >> 1) + 4 * h) * 2;
#pragma unroll
      for (int qd = 0; qd < 4; qd++) {
        u32x2 v = {pk2(z[4 * qd], z[4 * qd + 1]), pk2(z[4 * qd + 2], z[4 * qd + 3])};
        __builtin_amdgcn_raw_buffer_store_b64(v, rsO, ob + qd * 16, 0, 0);
      }
      STAMP(7);
    } else if (bid == 255 && wb == 3) {
      lds_wait(sm, 2, 4);
      for (;;) {
        u64 v[16];
#pragma unroll
        for (int i = 0; i < 16; i++) v[i] = __hip_atomic_load((const u64*)(ws + WS_FO + (i * 64 + lane) * FSTR), __ATOMIC_RELAXED, __HIP_MEMORY_SCOPE_AGENT);
        bool ok = true;
#pragma unroll
        for (int i = 0; i < 16; i++) ok = ok && v[i] == tag;
        if (__all(ok)) break;
        __builtin_amdgcn_s_sleep(8);
      }
      if (lane == 0) __hip_atomic_store((u64*)(ws + WS_EPOCH), tag, __ATOMIC_RELAXED, __HIP_MEMORY_SCOPE_AGENT);
      STAMP(10);
    }
  }
  STAMPS_OUT();
}

extern "C" void kernel_launch(void* const* d_in, const int* in_sizes, int n_in, void* d_out, int out_size, void* d_ws, size_t ws_size, hipStream_t stream) {
  Params P;
  P.x = (const char*)d_in[0]; P.wq = (const char*)d_in[1]; P.wk = (const char*)d_in[2]; P.wv = (const char*)d_in[3]; P.wo = (const char*)d_in[4];
  P.wg = (const char*)d_in[6]; P.wu = (const char*)d_in[7]; P.wd = (const char*)d_in[8]; P.gw = (const char*)d_in[9];
  P.out = (char*)d_out; P.ws = (char*)d_ws;
  fused_kernel<<<256, 512, 0, stream>>>(P);
}
```
